```python
import torch

def _softmax(x, axis=-1):
    return torch.softmax(x, dim=axis)
def _gelu(x):
    return torch.nn.functional.gelu(x, approximate="tanh")
def _silu(x):
    return torch.nn.functional.silu(x)
def _sigmoid(x):
    return torch.sigmoid(x)
def _layernorm(x, axis=-1, eps=1e-5):
    m = x.mean(dim=axis, keepdim=True)
    v = x.var(dim=axis, keepdim=True, unbiased=False)
    return (x - m) / torch.sqrt(v + eps)
def _scatter_add(tgt, idx, src, axis=0):
    return tgt.clone().index_add_(axis, idx, src)
def _segment_sum(src, seg, n):
    out = torch.zeros((n,) + src.shape[1:], dtype=src.dtype)
    return out.index_add_(0, seg, src)
def _pad(x, pw):
    flat = tuple(v for pair in reversed(pw) for v in pair)
    return torch.nn.functional.pad(x, flat)

kernel_name = 'recurrent_scan__compressed_kv_cla__dense_activation_squared_relu__decode'

def setup_inputs():

    _w = (lambda a: (lambda t: t.to(torch.bfloat16) if t.is_floating_point() else t)(torch.as_tensor(a)))
    def _A_setup():
        B, H, Sq, Sk, D, M = 1, 16, 1, 8192, 256, 448
        Dc = 4 * D
        return {
            "x": _w((lambda *s: torch.randn(*s, dtype=torch.bfloat16) * 0.05)(B, Sq, M)),
            "c_kv": _w((lambda *s: torch.randn(*s, dtype=torch.bfloat16) * 0.8)(B, Sk, Dc)),
            "w_q": _w((lambda *s: torch.randn(*s, dtype=torch.bfloat16) * 1.6)(M, H, D) * (M ** -0.5)),
            "w_uk": _w((lambda *s: torch.randn(*s, dtype=torch.bfloat16) * 1.6)(Dc, H, D) * (Dc ** -0.5)),
            "w_uv": _w((lambda *s: torch.randn(*s, dtype=torch.bfloat16) * 0.8)(Dc, H, D) * (Dc ** -0.5)),
            "w_o": _w((lambda *s: torch.randn(*s, dtype=torch.bfloat16) * 0.8)(H, D, M) * ((H * D) ** -0.5)),
        }
    _A_in = _A_setup()

    def _B_setup():
        B, S, D, F = 1, 1, 448, 1792
        return {
            "x": _w((lambda *s: torch.randn(*s, dtype=torch.bfloat16) * 0.05)(B, S, D)),
            "w1": _w((lambda *s: torch.randn(*s, dtype=torch.bfloat16) * 0.2)(D, F)),
            "b1": _w((lambda *s: torch.randn(*s, dtype=torch.bfloat16) * 0.2)(F)),
            "w2": _w((lambda *s: torch.randn(*s, dtype=torch.bfloat16) * 0.2)(F, D)),
            "b2": _w((lambda *s: torch.randn(*s, dtype=torch.bfloat16) * 0.2)(D)),
        }
    _B_in = _B_setup()

    return {
        "A_x": _A_in["x"],
        "A_c_kv": _A_in["c_kv"],
        "A_w_q": _A_in["w_q"],
        "A_w_uk": _A_in["w_uk"],
        "A_w_uv": _A_in["w_uv"],
        "A_w_o": _A_in["w_o"],
        "B_x": _B_in["x"],
        "B_w1": _B_in["w1"],
        "B_b1": _B_in["b1"],
        "B_w2": _B_in["w2"],
        "B_b2": _B_in["b2"],
    }

def reference(A_x, A_c_kv, A_w_q, A_w_uk, A_w_uv, A_w_o, B_x, B_w1, B_b1, B_w2, B_b2):
    def _A_ref(x, c_kv, w_q, w_uk, w_uv, w_o):
        B, Sq, M = x.shape[0], x.shape[1], x.shape[2]
        Sk = c_kv.shape[1]
        H, D = w_q.shape[1], w_q.shape[2]
        q = torch.einsum("bqm,mhd->bhqd", x, w_q)
        k = torch.einsum("bkc,chd->bhkd", c_kv, w_uk)
        v = torch.einsum("bkc,chd->bhkd", c_kv, w_uv)
        scores = torch.einsum("bhqd,bhkd->bhqk", q, k) * (D ** -0.5)
        off = Sk - Sq
        qi = torch.arange(Sq)[:, None]
        kj = torch.arange(Sk)[None, :]
        cmask = kj <= (qi + off)
        scores = torch.where(cmask[None, None], scores, -1e9)
        p = _softmax(scores, axis=-1)
        ctx = torch.einsum("bhqk,bhkd->bhqd", p, v)
        return torch.einsum("bhqd,hdm->bqm", ctx, w_o)

    def _B_ref(x, w1, b1, w2, b2):
        h = torch.einsum("bsd,df->bsf", x, w1) + b1
        zero = torch.zeros_like(h)
        r = torch.where(h > zero, h, zero)
        h = r * r
        out = torch.einsum("bsf,fd->bsd", h, w2) + b2
        return out

    x = A_x
    xn = _layernorm(x, axis=-1)
    s = _A_ref(xn, A_c_kv, A_w_q, A_w_uk, A_w_uv, A_w_o)
    g = _silu(_B_ref(xn, B_w1, B_b1, B_w2, B_b2))
    return x + s * g
```

```cpp
#include <hip/hip_runtime.h>
#include <hip/hip_bf16.h>
#include <stdint.h>

typedef unsigned short u16;
typedef unsigned v4u __attribute__((ext_vector_type(4)));

constexpr int H = 16, SK = 8192, D = 256, M = 448, DC = 1024, F = 1792;

__device__ __forceinline__ float bf2f(unsigned b) { return __builtin_bit_cast(float, b << 16); }
__device__ __forceinline__ float bflo(unsigned w) { return __builtin_bit_cast(float, w << 16); }
__device__ __forceinline__ float bfhi(unsigned w) { return __builtin_bit_cast(float, w & 0xffff0000u); }

__device__ __forceinline__ float wave_sum(float v) {
    for (int o = 32; o > 0; o >>= 1) v += __shfl_xor(v, o);
    return v;
}
__device__ __forceinline__ float wave_max(float v) {
    for (int o = 32; o > 0; o >>= 1) v = fmaxf(v, __shfl_xor(v, o));
    return v;
}

__device__ void layernorm_lds(const u16* x, float* xn, float* red) {
    int t = threadIdx.x;
    float a = 0.f, b = 0.f;
    float v0 = t < M ? bf2f(x[t]) : 0.f;
    float v1 = (t + 256) < M ? bf2f(x[t + 256]) : 0.f;
    float s = wave_sum(v0 + v1);
    if ((t & 63) == 0) red[t >> 6] = s;
    __syncthreads();
    float mean = (red[0] + red[1] + red[2] + red[3]) * (1.f / M);
    __syncthreads();
    a = t < M ? v0 - mean : 0.f;
    b = (t + 256) < M ? v1 - mean : 0.f;
    s = wave_sum(a * a + b * b);
    if ((t & 63) == 0) red[t >> 6] = s;
    __syncthreads();
    float var = (red[0] + red[1] + red[2] + red[3]) * (1.f / M);
    float rs = 1.f / sqrtf(var + 1e-5f);
    if (t < M) xn[t] = a * rs;
    if (t + 256 < M) xn[t + 256] = b * rs;
    __syncthreads();
}

__global__ void __launch_bounds__(256) k1(const u16* x, const u16* wq, const u16* w1, const u16* b1, float* q, float* hh) {
    __shared__ float xn[M];
    __shared__ float red[4];
    __shared__ float part[4][64];
    layernorm_lds(x, xn, red);
    int b = blockIdx.x, t = threadIdx.x;
    int col = (t & 63), mq = t >> 6;
    if (b < 64) {
        int c = b * 64 + col;
        float acc = 0.f;
        for (int m = mq * 112; m < mq * 112 + 112; ++m) acc += xn[m] * bf2f(wq[(size_t)m * 4096 + c]);
        part[mq][col] = acc;
        __syncthreads();
        if (t < 64) q[c] = part[0][t] + part[1][t] + part[2][t] + part[3][t];
    } else {
        int c = (b - 64) * 64 + col;
        float acc = 0.f;
        for (int m = mq * 112; m < mq * 112 + 112; ++m) acc += xn[m] * bf2f(w1[(size_t)m * F + c]);
        part[mq][col] = acc;
        __syncthreads();
        if (t < 64) {
            float v = part[0][t] + part[1][t] + part[2][t] + part[3][t] + bf2f(b1[c]);
            v = v > 0.f ? v : 0.f;
            hh[c] = v * v;
        }
    }
}

__global__ void __launch_bounds__(256) k2(const float* q, const u16* wuk, const float* hh, const u16* w2, const u16* b2, float* qabs, float* g) {
    int b = blockIdx.x, t = threadIdx.x, lane = t & 63, w = t >> 6;
    if (b < 256) {
        int c = b * 4 + w;
        const v4u* row = (const v4u*)(wuk + (size_t)c * 4096);
        for (int i = 0; i < 8; ++i) {
            v4u v = row[i * 64 + lane];
            const float* qq = q + (i * 64 + lane) * 8;
            float acc = bflo(v.x) * qq[0] + bfhi(v.x) * qq[1] + bflo(v.y) * qq[2] + bfhi(v.y) * qq[3] + bflo(v.z) * qq[4] + bfhi(v.z) * qq[5] + bflo(v.w) * qq[6] + bfhi(v.w) * qq[7];
            for (int o = 16; o > 0; o >>= 1) acc += __shfl_xor(acc, o);
            if ((lane & 31) == 0) qabs[(2 * i + (lane >> 5)) * DC + c] = acc * 0.0625f;
        }
    } else {
        __shared__ float part[4][64];
        int c = (b - 256) * 64 + lane;
        float acc = 0.f;
        for (int f = w * 448; f < w * 448 + 448; ++f) acc += hh[f] * bf2f(w2[(size_t)f * M + c]);
        part[w][lane] = acc;
        __syncthreads();
        if (t < 64) {
            float v = part[0][t] + part[1][t] + part[2][t] + part[3][t] + bf2f(b2[c]);
            g[c] = v / (1.f + __expf(-v));
        }
    }
}

__global__ void __launch_bounds__(256) k3(const float* qabs, const u16* ckv, float* sc) {
    int b = blockIdx.x, t = threadIdx.x, lane = t & 63, w = t >> 6;
    for (int kk = 0; kk < 8; ++kk) {
        int k = b * 32 + w * 8 + kk;
        const v4u* row = (const v4u*)(ckv + (size_t)k * DC);
        v4u v0 = row[lane], v1 = row[64 + lane];
        float x[16] = {bflo(v0.x), bfhi(v0.x), bflo(v0.y), bfhi(v0.y), bflo(v0.z), bfhi(v0.z), bflo(v0.w), bfhi(v0.w),
                       bflo(v1.x), bfhi(v1.x), bflo(v1.y), bfhi(v1.y), bflo(v1.z), bfhi(v1.z), bflo(v1.w), bfhi(v1.w)};
        for (int h = 0; h < H; ++h) {
            const float* qa = qabs + h * DC + lane * 8;
            float acc = 0.f;
            for (int j = 0; j < 8; ++j) acc += x[j] * qa[j] + x[8 + j] * qa[512 + j];
            acc = wave_sum(acc);
            if (lane == 0) sc[h * SK + k] = acc;
        }
    }
}

__global__ void __launch_bounds__(256) k4(float* sc) {
    __shared__ float red[4];
    int h = blockIdx.x, t = threadIdx.x;
    float* s = sc + h * SK;
    float m = -1e30f;
    for (int k = t; k < SK; k += 256) m = fmaxf(m, s[k]);
    m = wave_max(m);
    if ((t & 63) == 0) red[t >> 6] = m;
    __syncthreads();
    m = fmaxf(fmaxf(red[0], red[1]), fmaxf(red[2], red[3]));
    __syncthreads();
    float l = 0.f;
    for (int k = t; k < SK; k += 256) { float e = __expf(s[k] - m); s[k] = e; l += e; }
    l = wave_sum(l);
    if ((t & 63) == 0) red[t >> 6] = l;
    __syncthreads();
    l = red[0] + red[1] + red[2] + red[3];
    float inv = 1.f / l;
    for (int k = t; k < SK; k += 256) s[k] *= inv;
}

__global__ void __launch_bounds__(256) k5(const float* p, const u16* ckv, float* part) {
    __shared__ float red[4][16][128];
    int kb = blockIdx.x >> 3, cb = blockIdx.x & 7, t = threadIdx.x, lane = t & 63, w = t >> 6;
    float acc[16][2];
    for (int h = 0; h < 16; ++h) acc[h][0] = acc[h][1] = 0.f;
    for (int kk = 0; kk < 64; ++kk) {
        int k = kb * 256 + w * 64 + kk;
        unsigned v = *(const unsigned*)(ckv + (size_t)k * DC + cb * 128 + lane * 2);
        float a = bflo(v), b = bfhi(v);
        for (int h = 0; h < 16; ++h) { float pp = p[h * SK + k]; acc[h][0] += pp * a; acc[h][1] += pp * b; }
    }
    for (int h = 0; h < 16; ++h) { red[w][h][lane * 2] = acc[h][0]; red[w][h][lane * 2 + 1] = acc[h][1]; }
    __syncthreads();
    for (int i = t; i < 16 * 128; i += 256) {
        int h = i >> 7, c = i & 127;
        part[((size_t)kb * 16 + h) * DC + cb * 128 + c] = red[0][h][c] + red[1][h][c] + red[2][h][c] + red[3][h][c];
    }
}

__global__ void __launch_bounds__(256) k6(const float* part, const u16* wuv, float* ctx) {
    __shared__ float cc[DC];
    __shared__ float red[4][64];
    int h = blockIdx.x >> 2, dq = blockIdx.x & 3, t = threadIdx.x, lane = t & 63, w = t >> 6;
    for (int c = t; c < DC; c += 256) {
        float s = 0.f;
        for (int kb = 0; kb < 32; ++kb) s += part[((size_t)kb * 16 + h) * DC + c];
        cc[c] = s;
    }
    __syncthreads();
    float acc = 0.f;
    for (int c = w * 256; c < w * 256 + 256; ++c) acc += cc[c] * bf2f(wuv[(size_t)c * 4096 + h * 256 + dq * 64 + lane]);
    red[w][lane] = acc;
    __syncthreads();
    if (t < 64) ctx[h * 256 + dq * 64 + t] = red[0][t] + red[1][t] + red[2][t] + red[3][t];
}

__global__ void __launch_bounds__(1024) k7(const float* ctx, const u16* wo, const float* g, const u16* x, __hip_bfloat16* out) {
    __shared__ float red[16][64];
    int t = threadIdx.x, lane = t & 63, w = t >> 6;
    int m = blockIdx.x * 64 + lane;
    float acc = 0.f;
    for (int j = w * 256; j < w * 256 + 256; ++j) acc += ctx[j] * bf2f(wo[(size_t)j * M + m]);
    red[w][lane] = acc;
    __syncthreads();
    if (t < 64) {
        float s = 0.f;
        for (int i = 0; i < 16; ++i) s += red[i][t];
        out[m] = __float2bfloat16(bf2f(x[m]) + s * g[m]);
    }
}

extern "C" void kernel_launch(void* const* d_in, const int* in_sizes, int n_in,
                              void* d_out, int out_size, void* d_ws, size_t ws_size,
                              hipStream_t stream) {
    const u16* x = (const u16*)d_in[0];
    const u16* ckv = (const u16*)d_in[1];
    const u16* wq = (const u16*)d_in[2];
    const u16* wuk = (const u16*)d_in[3];
    const u16* wuv = (const u16*)d_in[4];
    const u16* wo = (const u16*)d_in[5];
    const u16* w1 = (const u16*)d_in[7];
    const u16* b1 = (const u16*)d_in[8];
    const u16* w2 = (const u16*)d_in[9];
    const u16* b2 = (const u16*)d_in[10];
    float* ws = (float*)d_ws;
    float* q = ws;
    float* hh = q + 4096;
    float* qabs = hh + 2048;
    float* g = qabs + 16384;
    float* sc = g + 512;
    float* part = sc + 131072;
    float* ctx = part + 32 * 16 * 1024;
    k1<<<64 + 28, 256, 0, stream>>>(x, wq, w1, b1, q, hh);
    k2<<<256 + 7, 256, 0, stream>>>(q, wuk, hh, w2, b2, qabs, g);
    k3<<<256, 256, 0, stream>>>(qabs, ckv, sc);
    k4<<<16, 256, 0, stream>>>(sc);
    k5<<<256, 256, 0, stream>>>(sc, ckv, part);
    k6<<<64, 256, 0, stream>>>(part, wuv, ctx);
    k7<<<7, 1024, 0, stream>>>(ctx, wo, g, x, (__hip_bfloat16*)d_out);
}
```

```cpp
#include <hip/hip_runtime.h>
#include <hip/hip_bf16.h>

typedef unsigned short u16;
typedef unsigned long long u64;
typedef unsigned v4u __attribute__((ext_vector_type(4)));
typedef unsigned v2u __attribute__((ext_vector_type(2)));
typedef float f32x4 __attribute__((ext_vector_type(4)));
typedef float f32x2 __attribute__((ext_vector_type(2)));
typedef __bf16 bf16x8 __attribute__((ext_vector_type(8)));
typedef __bf16 bf16x2 __attribute__((ext_vector_type(2)));
typedef short s16x4 __attribute__((ext_vector_type(4)));
#define LAS __attribute__((address_space(3)))
#define GAS __attribute__((address_space(1)))
#define DEV __device__ __forceinline__
typedef __amdgpu_buffer_rsrc_t rsrc_t;

constexpr int WS_E = 0;
constexpr int WS_FLAG = 4096;
constexpr int WS_Q1 = 8192;
constexpr int WS_Q2 = WS_Q1 + 65536;
constexpr int WS_S3 = WS_Q2 + 32768;
constexpr int WS_P4 = WS_S3 + 262144;
constexpr int WS_ST4 = WS_P4 + 524288;
constexpr int WS_P5 = WS_ST4 + 4096;
constexpr int WS_M5 = WS_P5 + 14 * 16384;
constexpr int WS_H3 = WS_M5 + 14 * 112 * 128;
constexpr int WS_END = WS_Q2 + 128 * 10240;
static_assert(WS_H3 + 128 * 128 <= WS_END, "workspace map");
constexpr unsigned SENT = 0xffffffffu;
constexpr int SPIN_CAP = 1 << 14;
constexpr int OOB = 0x7ffffff0;
constexpr unsigned CODE_LINES = 145;

constexpr int LW_COLS = 0;
constexpr int LW_PF = 131072;
constexpr int LW_XN = LW_PF + 16384;
constexpr int LW_QP = LW_XN + 2048;
constexpr int LW_QV = LW_QP + 1024;
constexpr int LW_HP = LW_QV + 512;
constexpr int LW_FR = LW_HP + 512;
constexpr int LW_TR = LW_FR + 2048;
constexpr int LW_ST = LW_TR + 16 * 272;
constexpr int LW_QA = LW_ST + 128;
constexpr int LW_Z = LW_QA + 256;
constexpr int LW_CNT = LW_Z + 512;
#define RS 4
constexpr int LA_Q = 0;
constexpr int LA_RING = 32768;
constexpr int LA_WO = 0;
constexpr int LA_BN = LA_WO + 60416;
constexpr int LA_CT = LA_BN + 8 * 2304;
constexpr int LA_UV = 83968; static_assert(LA_RING + 4 * RS * 2048 <= 83968, "ring");
static_assert(LA_CT + 2304 <= LA_UV && LA_UV + 65536 <= 161280 - 256, "A LDS map");
constexpr int LDS_BYTES = 161280;

#define A_W0 1
#define A_SLOWN 2
#define EARLY_COLS 0
#define H1_SLEEP 8
#define H3_SLEEP 0
#define TAIL_X 4
#define COLS_WIN 16
#define H2_TICKS 280
#define FLAG_TICKS 150
#define STAMP(i) do {} while (0)
#define STAMP_OUT() do {} while (0)

DEV unsigned f2u(float f) { return __builtin_bit_cast(unsigned, f); }
DEV float u2f(unsigned u) { return __builtin_bit_cast(float, u); }
DEV float bflo(unsigned w) { return u2f(w << 16); }
DEV float bfhi(unsigned w) { return u2f(w & 0xffff0000u); }
DEV unsigned pk2(float a, float b) { f32x2 p = {a, b}; bf16x2 r = __builtin_convertvector(p, bf16x2); return __builtin_bit_cast(unsigned, r); }
DEV float rbf(float a) { return bflo(pk2(a, a)); }
DEV unsigned nosent(unsigned u) { return u < 0xfffffffeu ? u : 0xfffffffeu; }
DEV f32x4 mfma(v4u a, v4u b, f32x4 c) {
    return __builtin_amdgcn_mfma_f32_16x16x32_bf16(__builtin_bit_cast(bf16x8, a), __builtin_bit_cast(bf16x8, b), c, 0, 0, 0);
}
template <int CTRL> DEV float dpp(float v) { return u2f((unsigned)__builtin_amdgcn_update_dpp(0, (int)f2u(v), CTRL, 0xf, 0xf, true)); }
constexpr int DPP_X1 = 0xB1, DPP_X2 = 0x4E, DPP_ROR4 = 0x124, DPP_ROR8 = 0x128;
DEV void swap16(unsigned& a, unsigned& b) { auto r = __builtin_amdgcn_permlane16_swap(a, b, false, false); a = r[0]; b = r[1]; }
DEV void swap32(unsigned& a, unsigned& b) { auto r = __builtin_amdgcn_permlane32_swap(a, b, false, false); a = r[0]; b = r[1]; }
DEV float add16(float a, float b) { unsigned x = f2u(a), y = f2u(b); swap16(x, y); return u2f(x) + u2f(y); }
DEV float add32(float a, float b) { unsigned x = f2u(a), y = f2u(b); swap32(x, y); return u2f(x) + u2f(y); }
DEV float max16(float a) { unsigned x = f2u(a), y = x; swap16(x, y); return fmaxf(u2f(x), u2f(y)); }
DEV float max32(float a) { unsigned x = f2u(a), y = x; swap32(x, y); return fmaxf(u2f(x), u2f(y)); }
DEV float wave_sum(float v) {
    v += dpp<DPP_X1>(v); v += dpp<DPP_X2>(v); v += dpp<DPP_ROR4>(v); v += dpp<DPP_ROR8>(v);
    v = add16(v, v); v = add32(v, v); return v;
}
DEV v4u ldsc(rsrc_t rs, int off) { return __builtin_amdgcn_raw_buffer_load_b128(rs, off, 0, 16); }
DEV v2u ldsc2(rsrc_t rs, int off) { return __builtin_amdgcn_raw_buffer_load_b64(rs, off, 0, 16); }
DEV void stsc(rsrc_t rs, int off, v4u v) { __builtin_amdgcn_raw_buffer_store_b128(v, rs, off, 0, 16); }
DEV void stsc2(rsrc_t rs, int off, v2u v) { __builtin_amdgcn_raw_buffer_store_b64(v, rs, off, 0, 16); }
DEV unsigned umax4(v4u v) { return max(max(v.x, v.y), max(v.z, v.w)); }
DEV v4u ldg(const void* p) { return *(const GAS v4u*)p; }
template <int IMM> DEV void dmas(const void* base, unsigned voff, int ldst) {
    asm volatile("s_mov_b32 m0, %2\n\ts_nop 0\n\tglobal_load_lds_dwordx4 %0, %1 offset:%3" :: "v"(voff), "s"(base), "s"(ldst), "i"(IMM) : "memory");
}
template <int N> DEV void wait_vm() { asm volatile("s_waitcnt vmcnt(%0)" :: "i"(N) : "memory"); }
DEV void wait_lgkm() { asm volatile("s_waitcnt lgkmcnt(0)" ::: "memory"); }
DEV v4u ldgo(const void* base, unsigned off) { return *(const GAS v4u*)((const GAS char*)base + off); }
DEV v2u trread(LAS unsigned char* p) { return __builtin_bit_cast(v2u, __builtin_amdgcn_ds_read_tr16_b64_v4i16((LAS s16x4*)p)); }
DEV v4u lds128(LAS unsigned char* p) { return *(LAS v4u*)p; }
constexpr int n_issued(int i) { return i < 0 ? 0 : ((i + (i < A_SLOWN ? A_W0 : RS)) < 16 ? (i + (i < A_SLOWN ? A_W0 : RS)) : 16); }
template <int V> struct IC { static constexpr int value = V; };
template <int B, int E, class F> DEV void static_for(F&& f) { if constexpr (B < E) { f(IC<B>{}); static_for<B + 1, E>(f); } }
#define COMPILER_FENCE() asm volatile("" ::: "memory")
#define WAIT_VM(n) __builtin_amdgcn_s_waitcnt(0x0F70 | ((n) & 15) | (((n) >> 4) << 14))
#define DRAIN() asm volatile("s_waitcnt vmcnt(0)" ::: "memory")
DEV void bar() { COMPILER_FENCE(); __builtin_amdgcn_s_waitcnt(0xC07F); __builtin_amdgcn_s_barrier(); COMPILER_FENCE(); }
#define PIN() __builtin_amdgcn_sched_barrier(0)

__global__ void __launch_bounds__(512, 2)
mla_decode_kernel(const u16* __restrict__ x, const u16* __restrict__ ckv, const u16* __restrict__ wq, const u16* __restrict__ wuk,
                  const u16* __restrict__ wuv, const u16* __restrict__ wo, const u16* __restrict__ w1, const u16* __restrict__ b1,
                  const u16* __restrict__ w2, const u16* __restrict__ b2, u16* __restrict__ out, unsigned char* ws) {
    extern __shared__ __attribute__((aligned(16))) unsigned char smem[];
    LAS unsigned char* L = (LAS unsigned char*)smem;
    const int tid = threadIdx.x, lane = tid & 63;
    {
        u64 pc;
        const unsigned line = tid >> 1;
        asm volatile("s_getpc_b64 %0" : "=s"(pc));
        asm volatile("s_mov_b32 m0, %2\n\ts_nop 0\n\tglobal_load_lds_dword %0, %1" :: "v"((line < CODE_LINES ? line : 0u) * 128u), "s"(pc), "i"(LDS_BYTES - 256) : "memory");
    }
    const u64 t_start = __builtin_amdgcn_s_memrealtime();
    asm volatile("" :: "s"(x), "s"(ckv), "s"(wq), "s"(wuk), "s"(wuv), "s"(wo), "s"(w1), "s"(b1), "s"(w2), "s"(b2), "s"(out), "s"(ws));
    u64 epoch = *(const __attribute__((address_space(4))) u64*)(ws + WS_E);
    const int w = __builtin_amdgcn_readfirstlane(tid >> 6);
    const int b = blockIdx.x, xc = b & 7, ii = b >> 3;
    const int n16 = lane & 15, g4 = lane >> 4;
    const rsrc_t rs = __builtin_amdgcn_make_buffer_rsrc(ws, 0, WS_END, 0x00020000);
    const v4u SV = {SENT, SENT, SENT, SENT};
    const float L2E = 1.4426950408889634f;

    if (__builtin_expect(ii < 16, 1)) {
        const int hk = 2 * xc + (ii >> 3), e = ii & 7;
        const int hf = ii, ef = xc;
        const bool has_mlp = ii < 14;
        const int fb = 14 * xc + ii;
        const int wq7 = w < 7 ? w : 6;
        unsigned tag_lo, tag_hi;
        if (w < 4) {
            __builtin_amdgcn_s_setprio(3);
            const v4u xv = ldgo(x, (lane < 56 ? lane : 0) * 16);
            v4u qa[7];
            const unsigned oq = (224 * (ef & 1) + 56 * w + (lane >> 3)) * 8192 + hf * 512 + (ef >> 1) * 128 + (lane & 7) * 16;
#pragma unroll
            for (int u = 0; u < 7; ++u) qa[u] = ldgo(wq, oq + u * 8 * 8192);
            PIN();
            __builtin_amdgcn_s_barrier();
            PIN();
            STAMP(12);
            float xf[8] = {bflo(xv.x), bfhi(xv.x), bflo(xv.y), bfhi(xv.y), bflo(xv.z), bfhi(xv.z), bflo(xv.w), bfhi(xv.w)};
            float s = ((xf[0] + xf[1]) + (xf[2] + xf[3])) + ((xf[4] + xf[5]) + (xf[6] + xf[7]));
            if (lane >= 56) s = 0.f;
            const float mean = wave_sum(s) * (1.f / 448.f);
            float s2 = 0.f;
#pragma unroll
            for (int j = 0; j < 8; ++j) { float d = xf[j] - mean; s2 += d * d; }
            if (lane >= 56) s2 = 0.f;
            const float var = wave_sum(s2) * (1.f / 448.f);
            const float mb = rbf(mean);
            const float den = rbf(__builtin_amdgcn_sqrtf(rbf(rbf(var) + 1e-5f)));
            const float rden = __builtin_amdgcn_rcpf(den);
            if (lane < 56) {
                f32x4 o0, o1;
#pragma unroll
                for (int j = 0; j < 4; ++j) { o0[j] = rbf(rbf(xf[j] - mb) * rden); o1[j] = rbf(rbf(xf[4 + j] - mb) * rden); }
                *(LAS f32x4*)(L + LW_XN + lane * 32) = o0;
                *(LAS f32x4*)(L + LW_XN + lane * 32 + 16) = o1;
            }
            COMPILER_FENCE();
            float xm[7];
#pragma unroll
            for (int u = 0; u < 7; ++u) xm[u] = *(LAS float*)(L + LW_XN + (224 * (ef & 1) + 56 * w + 8 * u + (lane >> 3)) * 4);
            asm volatile("" : "+s"(epoch));
            const u64 tag = epoch * 6364136223846793005ull + 1442695040888963407ull;
            tag_lo = (unsigned)tag; tag_hi = (unsigned)(tag >> 32);
            PIN();
            STAMP(13);
            float acc[8];
#pragma unroll
            for (int j = 0; j < 8; ++j) acc[j] = 0.f;
#pragma unroll
            for (int u = 0; u < 7; ++u) {
                acc[0] += xm[u] * bflo(qa[u].x); acc[1] += xm[u] * bfhi(qa[u].x); acc[2] += xm[u] * bflo(qa[u].y); acc[3] += xm[u] * bfhi(qa[u].y);
                acc[4] += xm[u] * bflo(qa[u].z); acc[5] += xm[u] * bfhi(qa[u].z); acc[6] += xm[u] * bflo(qa[u].w); acc[7] += xm[u] * bfhi(qa[u].w);
                PIN();
            }
            float r0 = add32(acc[0], acc[4]), r1 = add32(acc[1], acc[5]), r2 = add32(acc[2], acc[6]), r3 = add32(acc[3], acc[7]);
            float s0 = add16(r0, r2), s1 = add16(r1, r3);
            s0 += dpp<DPP_ROR8>(s0); s1 += dpp<DPP_ROR8>(s1);
            if ((lane & 8) == 0) {
                f32x2 o = {s0, s1};
                *(LAS f32x2*)(L + LW_QP + w * 256 + (8 * (lane & 7) + 4 * (lane >> 5) + 2 * ((lane >> 4) & 1)) * 4) = o;
            }
            STAMP(1);
            wait_lgkm();
            if (lane == 0) __hip_atomic_fetch_add((LAS unsigned*)(L + LW_CNT), 1u, __ATOMIC_RELAXED, __HIP_MEMORY_SCOPE_WORKGROUP);
            const unsigned th = (tag_lo >> 8) | (tag_hi << 24);
            if (w == 0) {
                for (int spin = 0; spin < (1 << 20) && *(volatile LAS unsigned*)(L + LW_CNT) < 4u; ++spin) {}
                COMPILER_FENCE();
                f32x2 a = {0.f, 0.f};
#pragma unroll
                for (int v = 0; v < 4; ++v) a += *(LAS f32x2*)(L + LW_QP + v * 256 + (lane & 31) * 8);
                const float ax = a.x, ay = a.y;
                v4u gr = {(f2u(ax) >> 8) | (tag_lo << 24), th, (f2u(ay) >> 8) | (tag_lo << 24), th};
                stsc(rs, lane < 32 ? WS_Q1 + hf * 4096 + ef * 512 + lane * 16 : OOB, gr);
                STAMP(2);
            }
            __builtin_amdgcn_s_sleep(H1_SLEEP);
            {
                const int off = WS_Q1 + hf * 4096 + w * 1024 + lane * 8;
                v2u v0, v1;
                v2u fl = ldsc2(rs, WS_FLAG + (w * 32 + (lane & 31)) * 8);
                for (int spin = 0;; ++spin) {
                    v0 = ldsc2(rs, off); v1 = ldsc2(rs, off + 512);
                    const bool ok = ((v0.x ^ (tag_lo << 24)) | (v1.x ^ (tag_lo << 24))) < 0x1000000u && v0.y == th && v1.y == th;
                    if (__all(ok) || spin > SPIN_CAP) break;
                    COMPILER_FENCE();
                }
                for (int spin = 0; !__all(fl.x == tag_lo && fl.y == tag_hi) && spin < SPIN_CAP; ++spin) {
                    __builtin_amdgcn_s_sleep(8);
                    fl = ldsc2(rs, WS_FLAG + (w * 32 + (lane & 31)) * 8);
                    COMPILER_FENCE();
                }
                const float qv = u2f(v0.x << 8) + u2f(v1.x << 8);
                *(LAS u16*)(L + LW_QV + (w * 64 + lane) * 2) = (u16)pk2(qv, qv);
            }
            STAMP(3);
            __builtin_amdgcn_s_setprio(0);
            bar();
        } else {
            const int kw = w - 4;
            if (kw == 0) { const v2u z = {0u, 0u}; *(LAS v2u*)(L + LW_Z + lane * 8) = z; *(LAS unsigned*)(L + LW_CNT) = 0u; }
            PIN();
            bar();
            v4u wk[16];
            const unsigned ok = (128 * ef + 32 * kw + (n16 >> 2)) * 8192 + hf * 512 + g4 * 64 + (n16 & 3) * 16;
#pragma unroll
            for (int u = 0; u < 16; ++u) wk[u] = ldgo(wuk, ok + (u >> 1) * 4 * 8192 + (u & 1) * 256);
            PIN();
            asm volatile("" : "+s"(epoch));
            const u64 tag = epoch * 6364136223846793005ull + 1442695040888963407ull;
            tag_lo = (unsigned)tag; tag_hi = (unsigned)(tag >> 32);
            STAMP(1);
            bar();
            v4u qf[8];
#pragma unroll
            for (int u = 0; u < 8; ++u) qf[u] = lds128(L + ((n16 >> 2) == (u >> 1) ? LW_QV : LW_Z) + (u & 1) * 256 + g4 * 64 + (n16 & 3) * 16);
            PIN();
            f32x4 d0 = {0.f, 0.f, 0.f, 0.f}, d1 = {0.f, 0.f, 0.f, 0.f};
#pragma unroll
            for (int u = 0; u < 8; ++u) d0 = mfma(wk[u], qf[u], d0);
#pragma unroll
            for (int u = 0; u < 8; ++u) d1 = mfma(wk[8 + u], qf[u], d1);
            float v0 = (lane & 2) ? ((lane & 1) ? d0.w : d0.z) : ((lane & 1) ? d0.y : d0.x);
            float v1 = (lane & 2) ? ((lane & 1) ? d1.w : d1.z) : ((lane & 1) ? d1.y : d1.x);
            v0 += dpp<DPP_X1>(v0); v1 += dpp<DPP_X1>(v1); v0 += dpp<DPP_X2>(v0); v1 += dpp<DPP_X2>(v1);
            const float v = ((lane & 1) ? v1 : v0) * 0.0625f;
            unsigned vb = pk2(v, v) & 0xffffu;
            vb = vb < 0xfffeu ? vb : 0xfffeu;
            if ((lane & 2) == 0) *(LAS u16*)(L + LW_QA + (32 * kw + 16 * (lane & 1) + (n16 & 12) + g4) * 2) = (u16)vb;
            STAMP(3);
        }
        const u64 tag = ((u64)tag_hi << 32) | tag_lo;
        bar();
        if (w == 4) stsc(rs, lane < 16 ? WS_Q2 + hf * 2048 + ef * 256 + lane * 16 : OOB, lds128(L + LW_QA + n16 * 16));
        PIN();
        STAMP(4);
        {
            const unsigned vo = (512 * hk + 64 * w + g4) * 2048 + e * 256;
#pragma unroll
            for (int u = 0; u < 16; ++u) {
                if (u >= COLS_WIN) wait_vm<COLS_WIN - 1>();
                dmas<0>(ckv, vo + u * 8192 + ((n16 ^ ((g4 << 2) | (u & 3))) * 16), LW_COLS + (64 * w + 4 * u) * 256);
            }
        }
        PIN();
        STAMP(5);
        wait_vm<0>();
        bar();
        STAMP(6);
        v4u vf[16];
        {
            const int q = n16 >> 2, p = lane & 3;
#pragma unroll
            for (int t = 0; t < 2; ++t) {
                LAS unsigned char* base = L + LW_COLS + (8 * g4 + 4 * t + q) * 256 + (((2 * w + (p >> 1)) ^ ((q << 2) | ((2 * g4 + t) & 3))) * 16) + 8 * (p & 1);
#pragma unroll
                for (int ks = 0; ks < 16; ++ks) {
                    v2u r = trread(base + ks * 8192);
                    if (t == 0) { vf[ks].x = r.x; vf[ks].y = r.y; } else { vf[ks].z = r.x; vf[ks].w = r.y; }
                }
            }
        }
        PIN();
        const int off3 = WS_S3 + (hk * 32 + (lane >> 2)) * 512 + w * 64 + (lane & 3) * 16;
        if (w == 0) {
            for (int spin = 0; spin < SPIN_CAP; ++spin) {
                const unsigned hv = __builtin_amdgcn_raw_buffer_load_b32(rs, WS_H3 + (hk * 8 + (lane & 7)) * 128, 0, 16);
                if (__all(hv != SENT)) break;
                COMPILER_FENCE();
            }
            if (H3_SLEEP) __builtin_amdgcn_s_sleep(H3_SLEEP);
        }
        STAMP(14);
        bar();
        v4u sc0 = ldsc(rs, off3), sc1 = ldsc(rs, off3 + 16 * 512);
        for (int spin = 0; spin < SPIN_CAP; ++spin) {
            const bool ok0 = __all(umax4(sc0) != SENT), ok1 = __all(umax4(sc1) != SENT);
            if (ok0 && ok1) break;
            if (!ok0) sc0 = ldsc(rs, off3);
            if (!ok1) sc1 = ldsc(rs, off3 + 16 * 512);
            COMPILER_FENCE();
        }
        STAMP(7);
        float sm_m, sm_l;
        {
            float s[16] = {bflo(sc0.x), bfhi(sc0.x), bflo(sc0.y), bfhi(sc0.y), bflo(sc0.z), bfhi(sc0.z), bflo(sc0.w), bfhi(sc0.w),
                           bflo(sc1.x), bfhi(sc1.x), bflo(sc1.y), bfhi(sc1.y), bflo(sc1.z), bfhi(sc1.z), bflo(sc1.w), bfhi(sc1.w)};
            float m = s[0];
#pragma unroll
            for (int j = 1; j < 16; ++j) m = fmaxf(m, s[j]);
            m = fmaxf(m, dpp<DPP_X1>(m)); m = fmaxf(m, dpp<DPP_ROR4>(m)); m = fmaxf(m, dpp<DPP_ROR8>(m));
            m = max16(m); m = max32(m);
            const float mm = -m * L2E;
#pragma unroll
            for (int j = 0; j < 16; ++j) s[j] = __builtin_amdgcn_exp2f(__builtin_fmaf(s[j], L2E, mm));
            v4u p0 = {pk2(s[0], s[1]), pk2(s[2], s[3]), pk2(s[4], s[5]), pk2(s[6], s[7])};
            v4u p1 = {pk2(s[8], s[9]), pk2(s[10], s[11]), pk2(s[12], s[13]), pk2(s[14], s[15])};
            const int tile = lane >> 2, head = 2 * w + ((lane >> 1) & 1), gp = 2 * (tile & 1) + (lane & 1);
            LAS unsigned char* dst = L + LW_PF + (tile >> 1) * 1024 + head * 64 + ((gp ^ ((-(head >> 2)) & 3)) * 16);
            *(LAS v4u*)dst = p0;
            *(LAS v4u*)(dst + 8192) = p1;
            PIN();
            sm_m = m;
            sm_l = (((s[0] + s[1]) + (s[2] + s[3])) + ((s[4] + s[5]) + (s[6] + s[7]))) + (((s[8] + s[9]) + (s[10] + s[11])) + ((s[12] + s[13]) + (s[14] + s[15])));
        }
        bar();
        v4u m1[2], m2[2];
        unsigned b1v = 0;
        {
            v4u pf[16];
            LAS unsigned char* pb = L + LW_PF + n16 * 64 + ((g4 ^ ((-(n16 >> 2)) & 3)) * 16);
#pragma unroll
            for (int ks = 0; ks < 16; ++ks) pf[ks] = lds128(pb + ks * 1024);
            PIN();
            f32x4 d = {0.f, 0.f, 0.f, 0.f};
#pragma unroll
            for (int ks = 0; ks < 16; ++ks) d = mfma(vf[ks], pf[ks], d);
            float l = sm_l;
            l += dpp<DPP_X1>(l); l += dpp<DPP_ROR4>(l); l += dpp<DPP_ROR8>(l); l = add16(l, l); l = add32(l, l);
            if ((lane & 61) == 0) { v2u o = {nosent(f2u(sm_m)), nosent(f2u(l))}; *(LAS v2u*)(L + LW_ST + (2 * w + (lane >> 1)) * 8) = o; }
            v2u o = {nosent(pk2(d.x, d.y)), nosent(pk2(d.z, d.w))};
            *(LAS v2u*)(L + LW_TR + n16 * 272 + w * 32 + g4 * 8) = o;
        }
        bar();
        if (w < 4) stsc(rs, WS_P4 + ((4 * w + g4) * 16 + hk) * 2048 + e * 256 + n16 * 16, lds128(L + LW_TR + (4 * w + g4) * 272 + n16 * 16));
        else if (w == 4 && e == 0) { if (lane < 8) stsc(rs, WS_ST4 + hk * 128 + lane * 16, lds128(L + LW_ST + lane * 16)); }
        PIN();
        STAMP(8);
        if (has_mlp) {
            {
                const char* p1 = (const char*)w1 + (size_t)(64 * wq7 + (lane >> 1)) * 3584 + fb * 32 + (lane & 1) * 16;
                m1[0] = ldg(p1); m1[1] = ldg(p1 + 32 * 3584);
                const char* p2 = (const char*)w2 + (size_t)(fb * 16 + 2 * w) * 896 + (lane < 56 ? lane : 0) * 16;
                m2[0] = ldg(p2); m2[1] = ldg(p2 + 896);
                b1v = *(const GAS unsigned*)((const char*)b1 + fb * 32 + w * 4);
            }
            PIN();
            if (w < 7) {
                float acc[8];
#pragma unroll
                for (int j = 0; j < 8; ++j) acc[j] = 0.f;
#pragma unroll
                for (int u = 0; u < 2; ++u) {
                    const float xm = *(LAS float*)(L + LW_XN + (64 * w + 32 * u + (lane >> 1)) * 4);
                    acc[0] += xm * bflo(m1[u].x); acc[1] += xm * bfhi(m1[u].x); acc[2] += xm * bflo(m1[u].y); acc[3] += xm * bfhi(m1[u].y);
                    acc[4] += xm * bflo(m1[u].z); acc[5] += xm * bfhi(m1[u].z); acc[6] += xm * bflo(m1[u].w); acc[7] += xm * bfhi(m1[u].w);
                }
                float r0 = add32(acc[0], acc[4]), r1 = add32(acc[1], acc[5]), r2 = add32(acc[2], acc[6]), r3 = add32(acc[3], acc[7]);
                float s0 = add16(r0, r2), s1 = add16(r1, r3);
                s0 += dpp<DPP_X2>(s0); s1 += dpp<DPP_X2>(s1);
                s0 += dpp<DPP_ROR4>(s0); s1 += dpp<DPP_ROR4>(s1); s0 += dpp<DPP_ROR8>(s0); s1 += dpp<DPP_ROR8>(s1);
                if ((lane & 14) == 0) {
                    f32x2 o = {s0, s1};
                    *(LAS f32x2*)(L + LW_HP + w * 64 + (8 * (lane & 1) + 4 * (lane >> 5) + 2 * ((lane >> 4) & 1)) * 4) = o;
                }
            }
            bar();
            f32x2 hh = {0.f, 0.f};
#pragma unroll
            for (int v = 0; v < 7; ++v) hh += *(LAS f32x2*)(L + LW_HP + v * 64 + w * 8);
            float h0 = rbf(rbf(hh.x) + bflo(b1v)), h1 = rbf(rbf(hh.y) + bfhi(b1v));
            h0 = h0 > 0.f ? h0 : 0.f; h1 = h1 > 0.f ? h1 : 0.f;
            h0 = rbf(h0 * h0); h1 = rbf(h1 * h1);
            if (lane < 56) {
                f32x4 o0 = {h0 * bflo(m2[0].x) + h1 * bflo(m2[1].x), h0 * bfhi(m2[0].x) + h1 * bfhi(m2[1].x),
                            h0 * bflo(m2[0].y) + h1 * bflo(m2[1].y), h0 * bfhi(m2[0].y) + h1 * bfhi(m2[1].y)};
                f32x4 o1 = {h0 * bflo(m2[0].z) + h1 * bflo(m2[1].z), h0 * bfhi(m2[0].z) + h1 * bfhi(m2[1].z),
                            h0 * bflo(m2[0].w) + h1 * bflo(m2[1].w), h0 * bfhi(m2[0].w) + h1 * bfhi(m2[1].w)};
                *(LAS f32x4*)(L + LW_COLS + w * 1792 + lane * 32) = o0;
                *(LAS f32x4*)(L + LW_COLS + w * 1792 + lane * 32 + 16) = o1;
            }
            bar();
            if (tid < 112) {
                f32x4 a = {0.f, 0.f, 0.f, 0.f};
#pragma unroll
                for (int v = 0; v < 8; ++v) a += *(LAS f32x4*)(L + LW_COLS + v * 1792 + tid * 16);
                float a0 = a.x, a1 = a.y, a2 = a.z, a3 = a.w;
                v4u o = {nosent(f2u(a0)), nosent(f2u(a1)), nosent(f2u(a2)), nosent(f2u(a3))};
                stsc(rs, WS_M5 + (tid >> 3) * (112 * 128) + fb * 128 + (tid & 7) * 16, o);
            }
            STAMP(9);
        } else {
            const int fi = 2 * xc + (ii - 14);
            if (fi < 14) {
                const unsigned xb = *(const GAS u16*)(x + fi * 32 + (lane & 31)), b2b = *(const GAS u16*)(b2 + fi * 32 + (lane & 31));
                const int offm = WS_M5 + fi * (112 * 128) + wq7 * 2048 + lane * 16, offs = WS_P5 + fi * 16384 + w * 2048 + lane * 16;
                v4u a0 = ldsc(rs, offm), a1 = ldsc(rs, offm + 1024), c0 = ldsc(rs, offs), c1 = ldsc(rs, offs + 1024);
                for (int spin = 0; spin < SPIN_CAP; ++spin) {
                    const bool k0 = __all(umax4(a0) != SENT), k1 = __all(umax4(a1) != SENT), k2 = __all(umax4(c0) != SENT), k3 = __all(umax4(c1) != SENT);
                    if (k0 && k1 && k2 && k3) break;
                    if (!k0) a0 = ldsc(rs, offm);
                    if (!k1) a1 = ldsc(rs, offm + 1024);
                    if (!k2) c0 = ldsc(rs, offs);
                    if (!k3) c1 = ldsc(rs, offs + 1024);
                    COMPILER_FENCE();
                }
                STAMP(10);
                {
                    const f32x4 am = __builtin_bit_cast(f32x4, a0) + __builtin_bit_cast(f32x4, a1);
                    const f32x4 as = __builtin_bit_cast(f32x4, c0) + __builtin_bit_cast(f32x4, c1);
                    float s0 = add16(add32(am.x, am.z), add32(am.y, am.w)), s1 = add16(add32(as.x, as.z), add32(as.y, as.w));
                    s0 += dpp<DPP_ROR8>(s0); s1 += dpp<DPP_ROR8>(s1);
                    f32x2 o = {s0, s1};
                    if ((lane & 8) == 0) *(LAS f32x2*)(L + LW_FR + w * 256 + ((lane & 7) * 4 + 2 * (lane >> 5) + ((lane >> 4) & 1)) * 8) = o;
                }
                bar();
                if (tid < 32) {
                    f32x2 a = {0.f, 0.f};
#pragma unroll
                    for (int v = 0; v < 7; ++v) a += *(LAS f32x2*)(L + LW_FR + v * 256 + tid * 8);
                    const float sv = a.y + *(LAS float*)(L + LW_FR + 7 * 256 + tid * 8 + 4);
                    const float o2 = rbf(rbf(a.x) + bflo(b2b));
                    const float gate = rbf(o2 * __builtin_amdgcn_rcpf(1.0f + __builtin_amdgcn_exp2f(-o2 * L2E)));
                    const float o = bflo(xb) + rbf(rbf(sv) * gate);
                    *(GAS u16*)(out + fi * 32 + tid) = (u16)pk2(o, o);
                    if (fi == 0 && tid == 0) *(GAS u64*)(ws + WS_E) = tag;
                }
                STAMP(11);
            }
        }
    } else {
        const int ia = ii - 16, ai = xc * 16 + ia;
        const int hd = 2 * xc + (ia >> 3), ch = (ia >> 2) & 1, dq = ia & 3;
        if (w < 4) {
            v4u a[32], q[16];
            const int r8 = lane >> 3, pos = lane & 7;
            const unsigned vo0 = (64 * ai + 16 * w + r8) * 2048 + ((pos ^ (r8 >> 1)) * 16);
            const unsigned vo1 = (64 * ai + 16 * w + 8 + r8) * 2048 + ((pos ^ (4 + (r8 >> 1))) * 16);
            const int ring = LA_RING + w * RS * 2048;
            LAS unsigned char* ra0 = L + ring + n16 * 128 + ((g4 ^ (n16 >> 1)) * 16);
            LAS unsigned char* ra1 = L + ring + n16 * 128 + (((4 + g4) ^ (n16 >> 1)) * 16);
            LAS unsigned char* qb = L + LA_Q + n16 * 64 + ((g4 ^ ((-(n16 >> 2)) & 3)) * 16);
            f32x4 d = {0.f, 0.f, 0.f, 0.f};
            static_for<0, 16>([&](auto I) {
                constexpr int i = I.value;
                static_for<n_issued(i - 1), n_issued(i)>([&](auto J) {
                    constexpr int j = J.value;
                    dmas<j * 128>(ckv, vo0, ring + (j % RS) * 2048 - j * 128);
                    dmas<j * 128>(ckv, vo1, ring + (j % RS) * 2048 + 1024 - j * 128);
                });
                if constexpr (n_issued(i) == 16 && n_issued(i - 1) < 16) {
                    STAMP(1);
                    bar();
                    STAMP(2);
#pragma unroll
                    for (int j = 0; j < 16; ++j) q[j] = lds128(qb + j * 1024);
                    PIN();
#pragma unroll
                    for (int j = 0; j < 16; ++j) d = mfma(a[j], q[j], d);
                    PIN();
#pragma unroll
                    for (int j = 0; j < 16; ++j) q[j] = lds128(qb + (16 + j) * 1024);
                    PIN();
#pragma unroll
                    for (int j = 16; j < 2 * i; ++j) d = mfma(a[j], q[j - 16], d);
                    PIN();
                }
                wait_vm<2 * (n_issued(i) - i - 1)>();
                a[2 * i] = lds128(ra0 + (i % RS) * 2048);
                a[2 * i + 1] = lds128(ra1 + (i % RS) * 2048);
                if constexpr (n_issued(i) < 16) {
                    asm volatile("s_waitcnt lgkmcnt(0)" : "+v"(a[2 * i]), "+v"(a[2 * i + 1]) :: "memory");
                } else {
                    d = mfma(a[2 * i], q[2 * i - 16], d);
                    d = mfma(a[2 * i + 1], q[2 * i + 1 - 16], d);
                    PIN();
                }
            });
            unsigned a0 = nosent(pk2(d.x, d.y)), a1 = nosent(pk2(d.z, d.w)), c0 = a0, c1 = a1;
            swap16(a0, c0); swap16(a1, c1);
            v4u o = {a0, a1, c0, c1};
            stsc(rs, (g4 & 1) == 0 ? WS_S3 + (ai * 4 + w) * 512 + n16 * 32 + (g4 >> 1) * 16 : OOB, o);
            PIN();
            STAMP(3);
        } else {
            const int tw = w - 4;
            if (tw == 0) {
#pragma unroll
                for (int u = 0; u < 10; ++u) stsc(rs, WS_Q2 + ai * 10240 + u * 1024 + lane * 16, SV);
            }
            PIN();
            asm volatile("" : "+s"(epoch));
            const u64 tag = epoch * 6364136223846793005ull + 1442695040888963407ull;
            const unsigned tag_lo = (unsigned)tag, tag_hi = (unsigned)(tag >> 32);
            if (tw == 0) {
                wait_vm<0>();
                v2u t = {tag_lo, tag_hi};
                stsc2(rs, lane == 0 ? WS_FLAG + ai * 8 : OOB, t);
            }
            STAMP(1);
            while (__builtin_amdgcn_s_memrealtime() - t_start < FLAG_TICKS) __builtin_amdgcn_s_sleep(2);
            for (int spin = 0;; ++spin) {
                v4u v = ldsc(rs, WS_FLAG + lane * 16);
                const bool ok = v.x == tag_lo && v.y == tag_hi && v.z == tag_lo && v.w == tag_hi;
                if (__all(ok) || spin > SPIN_CAP) break;
                __builtin_amdgcn_s_sleep(8);
                COMPILER_FENCE();
            }
            STAMP(2);
            while (__builtin_amdgcn_s_memrealtime() - t_start < H2_TICKS) __builtin_amdgcn_s_sleep(1);
            for (int spin = 0; spin < SPIN_CAP; ++spin) {
                v4u v = ldsc(rs, WS_Q2 + tw * 8192 + (ia & 7) * 1024 + lane * 16);
                if (__all(umax4(v) != SENT)) break;
                COMPILER_FENCE();
            }
            STAMP(12);
            {
                v4u qv[8];
                const int off = WS_Q2 + tw * 8192 + lane * 16;
#pragma unroll
                for (int u = 0; u < 8; ++u) qv[u] = ldsc(rs, off + u * 1024);
                for (int spin = 0; spin < SPIN_CAP; ++spin) {
                    bool all = true;
#pragma unroll
                    for (int u = 0; u < 8; ++u) {
                        if (!__all(umax4(qv[u]) != SENT)) { all = false; qv[u] = ldsc(rs, off + u * 1024); }
                    }
                    if (all) break;
                    COMPILER_FENCE();
                }
                LAS unsigned char* dst = L + LA_Q + (lane >> 2) * 1024 + tw * 256 + (((lane & 3) ^ ((-tw) & 3)) * 16);
#pragma unroll
                for (int u = 0; u < 8; ++u) *(LAS v4u*)(dst + (u & 1) * 16384 + (u >> 1) * 64) = qv[u];
            }
            STAMP(3);
            bar();
            if (tw == 0) __builtin_amdgcn_raw_buffer_store_b32(0u, rs, lane == 0 ? WS_H3 + ai * 128 : OOB, 0, 16);
            {
                const int r8 = lane >> 3, pos = lane & 7;
                const unsigned vo = (512 * ch + 128 * tw + r8) * 8192 + hd * 512 + dq * 128;
#pragma unroll
                for (int u = 0; u < TAIL_X; ++u)
                    dmas<0>(wuv, vo + u * 65536 + ((pos ^ ((((r8 >> 1) & 1) | ((u & 1) << 1)) << 1)) * 16), LA_UV + (128 * tw + 8 * u) * 128);
            }
            STAMP(4);
        }
        bar();
        {
            const int r8 = lane >> 3, pos = lane & 7, tq = w & 3, th = w >> 2;
            const unsigned vo = (512 * ch + 128 * tq + r8) * 8192 + hd * 512 + dq * 128;
#pragma unroll
            for (int v = 0; v < (16 - TAIL_X) / 2; ++v) {
                const int u = TAIL_X + 2 * v + th;
                dmas<0>(wuv, vo + u * 65536 + ((pos ^ ((((r8 >> 1) & 1) | ((u & 1) << 1)) << 1)) * 16), LA_UV + (128 * tq + 8 * u) * 128);
            }
            const unsigned so = (hd * 256 + dq * 64 + 8 * w) * 896 + lane * 16;
            if (lane < 56) {
#pragma unroll
                for (int u = 0; u < 8; ++u) dmas<0>(wo, so + u * 896, LA_WO + (8 * w + u) * 928 + w * 128);
            }
        }
        PIN();
        wait_vm<0>();
        STAMP(5);
        bar();
        const int off4 = WS_P4 + (hd * 16 + (lane >> 3)) * 2048 + ch * 1024 + w * 128 + (lane & 7) * 16;
        const int soff = WS_ST4 + g4 * 512 + hd * 8;
        v2u s0 = ldsc2(rs, soff), s1 = ldsc2(rs, soff + 128), s2 = ldsc2(rs, soff + 256), s3 = ldsc2(rs, soff + 384);
        v4u c0 = ldsc(rs, off4), c1 = ldsc(rs, off4 + 8 * 2048);
        PIN();
        v4u uf[8], of[8];
        {
            const int q = n16 >> 2, p = lane & 3;
#pragma unroll
            for (int t = 0; t < 2; ++t) {
                const int sw = ((q >> 1) | ((g4 & 1) << 1)) << 1;
                LAS unsigned char* ub = L + LA_UV + (64 * w + 8 * g4 + 4 * t + q) * 128 + 8 * (p & 1);
                const int wq7 = w < 7 ? w : 6;
                LAS unsigned char* ob = L + LA_WO + (8 * g4 + 4 * t + q) * 928 + g4 * 128 + wq7 * 128 + p * 8;
#pragma unroll
                for (int ks = 0; ks < 2; ++ks)
#pragma unroll
                    for (int ct = 0; ct < 4; ++ct) {
                        v2u r = trread(ub + ks * 4096 + (((2 * ct + (p >> 1)) ^ sw) * 16));
                        v2u o = trread(ob + ks * (32 * 928 + 512) + ct * 32);
                        if (t == 0) { uf[ks * 4 + ct].x = r.x; uf[ks * 4 + ct].y = r.y; of[ks * 4 + ct].x = o.x; of[ks * 4 + ct].y = o.y; }
                        else { uf[ks * 4 + ct].z = r.x; uf[ks * 4 + ct].w = r.y; of[ks * 4 + ct].z = o.x; of[ks * 4 + ct].w = o.y; }
                    }
            }
        }
        PIN();
        STAMP(6);
        {
            for (int spin = 0; spin < SPIN_CAP; ++spin) {
                const bool ks = __all(max(max(max(s0.x, s0.y), max(s1.x, s1.y)), max(max(s2.x, s2.y), max(s3.x, s3.y))) != SENT);
                const bool kc = __all(max(umax4(c0), umax4(c1)) != SENT);
                if (ks && kc) break;
                if (!ks) { s0 = ldsc2(rs, soff); s1 = ldsc2(rs, soff + 128); s2 = ldsc2(rs, soff + 256); s3 = ldsc2(rs, soff + 384); }
                if (!kc) { c0 = ldsc(rs, off4); c1 = ldsc(rs, off4 + 8 * 2048); }
                COMPILER_FENCE();
            }
            STAMP(7);
            {
                LAS unsigned char* bn = L + LA_BN + w * 2304;
                *(LAS v4u*)(bn + (lane >> 3) * 144 + (lane & 7) * 16) = c0;
                *(LAS v4u*)(bn + (8 + (lane >> 3)) * 144 + (lane & 7) * 16) = c1;
                COMPILER_FENCE();
                c0 = lds128(bn + n16 * 144 + g4 * 16);
                c1 = lds128(bn + n16 * 144 + 64 + g4 * 16);
            }
            f32x4 d[4];
#pragma unroll
            for (int ct = 0; ct < 4; ++ct) {
                f32x4 z = {0.f, 0.f, 0.f, 0.f};
                d[ct] = mfma(c0, uf[ct], z);
            }
#pragma unroll
            for (int ct = 0; ct < 4; ++ct) d[ct] = mfma(c1, uf[4 + ct], d[ct]);
            const float m0 = u2f(s0.x), m1 = u2f(s1.x), m2 = u2f(s2.x), m3 = u2f(s3.x);
            float M = fmaxf(fmaxf(m0, m1), fmaxf(m2, m3));
            M = max16(M); M = max32(M);
            float f0 = __builtin_amdgcn_exp2f((m0 - M) * L2E), f1 = __builtin_amdgcn_exp2f((m1 - M) * L2E);
            float f2 = __builtin_amdgcn_exp2f((m2 - M) * L2E), f3 = __builtin_amdgcn_exp2f((m3 - M) * L2E);
            float Ls = (f0 * u2f(s0.y) + f1 * u2f(s1.y)) + (f2 * u2f(s2.y) + f3 * u2f(s3.y));
            Ls = add16(Ls, Ls); Ls = add32(Ls, Ls);
            const float rl = __builtin_amdgcn_rcpf(Ls);
            f0 *= rl; f1 *= rl; f2 *= rl; f3 *= rl;
            float cx[4];
#pragma unroll
            for (int ct = 0; ct < 4; ++ct) cx[ct] = (f0 * d[ct].x + f1 * d[ct].y) + (f2 * d[ct].z + f3 * d[ct].w);
            const float r0 = add32(cx[0], cx[2]), r1 = add32(cx[1], cx[3]);
            const float cv = add16(r0, r1);
            const float hi = rbf(cv), lo = cv - hi;
            const unsigned pk = pk2(hi, lo);
            *(LAS u16*)(L + LA_CT + w * 144 + lane * 2) = (u16)pk;
            *(LAS u16*)(L + LA_CT + (8 + w) * 144 + lane * 2) = (u16)(pk >> 16);
        }
        bar();
        if (w < 7) {
            const v4u a0 = lds128(L + LA_CT + n16 * 144 + g4 * 16), a1 = lds128(L + LA_CT + n16 * 144 + 64 + g4 * 16);
            f32x4 d[4];
#pragma unroll
            for (int ct = 0; ct < 4; ++ct) {
                f32x4 z = {0.f, 0.f, 0.f, 0.f};
                d[ct] = mfma(a0, of[ct], z);
            }
#pragma unroll
            for (int ct = 0; ct < 4; ++ct) d[ct] = mfma(a1, of[4 + ct], d[ct]);
            float sx[4];
#pragma unroll
            for (int ct = 0; ct < 4; ++ct) sx[ct] = (d[ct].x + d[ct].y) + (d[ct].z + d[ct].w);
            const float r0 = add32(sx[0], sx[2]), r1 = add32(sx[1], sx[3]);
            const float sv = add16(r0, r1);
            const unsigned su = nosent(f2u(sv));
            v4u o;
            o.x = su;
            o.y = (unsigned)__builtin_amdgcn_update_dpp(0, (int)su, 0x55, 0xf, 0xf, true);
            o.z = (unsigned)__builtin_amdgcn_update_dpp(0, (int)su, 0xAA, 0xf, 0xf, true);
            o.w = (unsigned)__builtin_amdgcn_update_dpp(0, (int)su, 0xFF, 0xf, 0xf, true);
            stsc(rs, (lane & 3) == 0 ? WS_P5 + (2 * w + (lane >> 5)) * 16384 + ai * 128 + (lane & 31) * 4 : OOB, o);
        }
        STAMP(8);
    }
    STAMP_OUT();
}

extern "C" void kernel_launch(void* const* d_in, const int* in_sizes, int n_in, void* d_out, int out_size, void* d_ws, size_t ws_size,
                              hipStream_t stream) {
    (void)hipFuncSetAttribute((const void*)mla_decode_kernel, hipFuncAttributeMaxDynamicSharedMemorySize, LDS_BYTES);
    mla_decode_kernel<<<256, 512, LDS_BYTES, stream>>>((const u16*)d_in[0], (const u16*)d_in[1], (const u16*)d_in[2], (const u16*)d_in[3],
                                                       (const u16*)d_in[4], (const u16*)d_in[5], (const u16*)d_in[7], (const u16*)d_in[8],
                                                       (const u16*)d_in[9], (const u16*)d_in[10], (u16*)d_out, (unsigned char*)d_ws);
}
```

```cpp
#define STAMPS
#include <hip/hip_runtime.h>
#include <hip/hip_bf16.h>

typedef unsigned short u16;
typedef unsigned long long u64;
typedef unsigned v4u __attribute__((ext_vector_type(4)));
typedef unsigned v2u __attribute__((ext_vector_type(2)));
typedef float f32x4 __attribute__((ext_vector_type(4)));
typedef float f32x2 __attribute__((ext_vector_type(2)));
typedef __bf16 bf16x8 __attribute__((ext_vector_type(8)));
typedef __bf16 bf16x2 __attribute__((ext_vector_type(2)));
typedef short s16x4 __attribute__((ext_vector_type(4)));
#define LAS __attribute__((address_space(3)))
#define GAS __attribute__((address_space(1)))
#define DEV __device__ __forceinline__
typedef __amdgpu_buffer_rsrc_t rsrc_t;

constexpr int WS_E = 0;
constexpr int WS_FLAG = 4096;
constexpr int WS_Q1 = 8192;
constexpr int WS_Q2 = WS_Q1 + 65536;
constexpr int WS_S3 = WS_Q2 + 32768;
constexpr int WS_P4 = WS_S3 + 262144;
constexpr int WS_ST4 = WS_P4 + 524288;
constexpr int WS_P5 = WS_ST4 + 4096;
constexpr int WS_M5 = WS_P5 + 14 * 16384;
constexpr int WS_H3 = WS_M5 + 14 * 112 * 128;
constexpr int WS_END = WS_Q2 + 128 * 10240;
static_assert(WS_H3 + 128 * 128 <= WS_END, "workspace map");
constexpr unsigned SENT = 0xffffffffu;
constexpr int SPIN_CAP = 1 << 14;
constexpr int OOB = 0x7ffffff0;
constexpr unsigned CODE_LINES = 145;

constexpr int LW_COLS = 0;
constexpr int LW_PF = 131072;
constexpr int LW_XN = LW_PF + 16384;
constexpr int LW_QP = LW_XN + 2048;
constexpr int LW_QV = LW_QP + 1024;
constexpr int LW_HP = LW_QV + 512;
constexpr int LW_FR = LW_HP + 512;
constexpr int LW_TR = LW_FR + 2048;
constexpr int LW_ST = LW_TR + 16 * 272;
constexpr int LW_QA = LW_ST + 128;
constexpr int LW_Z = LW_QA + 256;
constexpr int LW_CNT = LW_Z + 512;
#define RS 4
constexpr int LA_Q = 0;
constexpr int LA_RING = 32768;
constexpr int LA_WO = 0;
constexpr int LA_BN = LA_WO + 60416;
constexpr int LA_CT = LA_BN + 8 * 2304;
constexpr int LA_UV = 83968; static_assert(LA_RING + 4 * RS * 2048 <= 83968, "ring");
static_assert(LA_CT + 2304 <= LA_UV && LA_UV + 65536 <= 161280 - 256, "A LDS map");
constexpr int LDS_BYTES = 161280;

#define A_W0 1
#define A_SLOWN 2
#define EARLY_COLS 0
#define H1_SLEEP 8
#define H3_SLEEP 0
#define TAIL_X 4
#define COLS_WIN 16
#define H2_TICKS 280
#define FLAG_TICKS 150
constexpr size_t STAMP_OFF = 128ull << 20;
constexpr int NST = 16;
#define STAMP(i) do { __builtin_amdgcn_sched_barrier(0); asm volatile("s_memrealtime %0\n\ts_waitcnt lgkmcnt(0)" : "=s"(stv[i]) :: "memory"); __builtin_amdgcn_sched_barrier(0); } while (0)
#define STAMP_OUT() do { if (lane == 0) { _Pragma("unroll") for (int i_ = 0; i_ < 16; ++i_) stamps[i_] = stv[i_]; } } while (0)

DEV unsigned f2u(float f) { return __builtin_bit_cast(unsigned, f); }
DEV float u2f(unsigned u) { return __builtin_bit_cast(float, u); }
DEV float bflo(unsigned w) { return u2f(w << 16); }
DEV float bfhi(unsigned w) { return u2f(w & 0xffff0000u); }
DEV unsigned pk2(float a, float b) { f32x2 p = {a, b}; bf16x2 r = __builtin_convertvector(p, bf16x2); return __builtin_bit_cast(unsigned, r); }
DEV float rbf(float a) { return bflo(pk2(a, a)); }
DEV unsigned nosent(unsigned u) { return u < 0xfffffffeu ? u : 0xfffffffeu; }
DEV f32x4 mfma(v4u a, v4u b, f32x4 c) {
    return __builtin_amdgcn_mfma_f32_16x16x32_bf16(__builtin_bit_cast(bf16x8, a), __builtin_bit_cast(bf16x8, b), c, 0, 0, 0);
}
template <int CTRL> DEV float dpp(float v) { return u2f((unsigned)__builtin_amdgcn_update_dpp(0, (int)f2u(v), CTRL, 0xf, 0xf, true)); }
constexpr int DPP_X1 = 0xB1, DPP_X2 = 0x4E, DPP_ROR4 = 0x124, DPP_ROR8 = 0x128;
DEV void swap16(unsigned& a, unsigned& b) { auto r = __builtin_amdgcn_permlane16_swap(a, b, false, false); a = r[0]; b = r[1]; }
DEV void swap32(unsigned& a, unsigned& b) { auto r = __builtin_amdgcn_permlane32_swap(a, b, false, false); a = r[0]; b = r[1]; }
DEV float add16(float a, float b) { unsigned x = f2u(a), y = f2u(b); swap16(x, y); return u2f(x) + u2f(y); }
DEV float add32(float a, float b) { unsigned x = f2u(a), y = f2u(b); swap32(x, y); return u2f(x) + u2f(y); }
DEV float max16(float a) { unsigned x = f2u(a), y = x; swap16(x, y); return fmaxf(u2f(x), u2f(y)); }
DEV float max32(float a) { unsigned x = f2u(a), y = x; swap32(x, y); return fmaxf(u2f(x), u2f(y)); }
DEV float wave_sum(float v) {
    v += dpp<DPP_X1>(v); v += dpp<DPP_X2>(v); v += dpp<DPP_ROR4>(v); v += dpp<DPP_ROR8>(v);
    v = add16(v, v); v = add32(v, v); return v;
}
DEV v4u ldsc(rsrc_t rs, int off) { return __builtin_amdgcn_raw_buffer_load_b128(rs, off, 0, 16); }
DEV v2u ldsc2(rsrc_t rs, int off) { return __builtin_amdgcn_raw_buffer_load_b64(rs, off, 0, 16); }
DEV void stsc(rsrc_t rs, int off, v4u v) { __builtin_amdgcn_raw_buffer_store_b128(v, rs, off, 0, 16); }
DEV void stsc2(rsrc_t rs, int off, v2u v) { __builtin_amdgcn_raw_buffer_store_b64(v, rs, off, 0, 16); }
DEV unsigned umax4(v4u v) { return max(max(v.x, v.y), max(v.z, v.w)); }
DEV v4u ldg(const void* p) { return *(const GAS v4u*)p; }
template <int IMM> DEV void dmas(const void* base, unsigned voff, int ldst) {
    asm volatile("s_mov_b32 m0, %2\n\ts_nop 0\n\tglobal_load_lds_dwordx4 %0, %1 offset:%3" :: "v"(voff), "s"(base), "s"(ldst), "i"(IMM) : "memory");
}
template <int N> DEV void wait_vm() { asm volatile("s_waitcnt vmcnt(%0)" :: "i"(N) : "memory"); }
DEV void wait_lgkm() { asm volatile("s_waitcnt lgkmcnt(0)" ::: "memory"); }
DEV v4u ldgo(const void* base, unsigned off) { return *(const GAS v4u*)((const GAS char*)base + off); }
DEV v2u trread(LAS unsigned char* p) { return __builtin_bit_cast(v2u, __builtin_amdgcn_ds_read_tr16_b64_v4i16((LAS s16x4*)p)); }
DEV v4u lds128(LAS unsigned char* p) { return *(LAS v4u*)p; }
constexpr int n_issued(int i) { return i < 0 ? 0 : ((i + (i < A_SLOWN ? A_W0 : RS)) < 16 ? (i + (i < A_SLOWN ? A_W0 : RS)) : 16); }
template <int V> struct IC { static constexpr int value = V; };
template <int B, int E, class F> DEV void static_for(F&& f) { if constexpr (B < E) { f(IC<B>{}); static_for<B + 1, E>(f); } }
#define COMPILER_FENCE() asm volatile("" ::: "memory")
#define WAIT_VM(n) __builtin_amdgcn_s_waitcnt(0x0F70 | ((n) & 15) | (((n) >> 4) << 14))
#define DRAIN() asm volatile("s_waitcnt vmcnt(0)" ::: "memory")
DEV void bar() { COMPILER_FENCE(); __builtin_amdgcn_s_waitcnt(0xC07F); __builtin_amdgcn_s_barrier(); COMPILER_FENCE(); }
#define PIN() __builtin_amdgcn_sched_barrier(0)

__global__ void __launch_bounds__(512, 2)
mla_decode_kernel(const u16* __restrict__ x, const u16* __restrict__ ckv, const u16* __restrict__ wq, const u16* __restrict__ wuk,
                  const u16* __restrict__ wuv, const u16* __restrict__ wo, const u16* __restrict__ w1, const u16* __restrict__ b1,
                  const u16* __restrict__ w2, const u16* __restrict__ b2, u16* __restrict__ out, unsigned char* ws) {
    extern __shared__ __attribute__((aligned(16))) unsigned char smem[];
    LAS unsigned char* L = (LAS unsigned char*)smem;
    const int tid = threadIdx.x, lane = tid & 63;
    {
        u64 pc;
        const unsigned line = tid >> 1;
        asm volatile("s_getpc_b64 %0" : "=s"(pc));
        asm volatile("s_mov_b32 m0, %2\n\ts_nop 0\n\tglobal_load_lds_dword %0, %1" :: "v"((line < CODE_LINES ? line : 0u) * 128u), "s"(pc), "i"(LDS_BYTES - 256) : "memory");
    }
    const u64 t_start = __builtin_amdgcn_s_memrealtime();
    asm volatile("" :: "s"(x), "s"(ckv), "s"(wq), "s"(wuk), "s"(wuv), "s"(wo), "s"(w1), "s"(b1), "s"(w2), "s"(b2), "s"(out), "s"(ws));
    u64 epoch = *(const __attribute__((address_space(4))) u64*)(ws + WS_E);
    const int w = __builtin_amdgcn_readfirstlane(tid >> 6);
    const int b = blockIdx.x, xc = b & 7, ii = b >> 3;
    const int n16 = lane & 15, g4 = lane >> 4;
    const rsrc_t rs = __builtin_amdgcn_make_buffer_rsrc(ws, 0, WS_END, 0x00020000);
    const v4u SV = {SENT, SENT, SENT, SENT};
    const float L2E = 1.4426950408889634f;
    u64* stamps = (u64*)(ws + STAMP_OFF) + (size_t)(b * 8 + w) * NST;
    u64 stv[16] = {0, 0, 0, 0, 0, 0, 0, 0, 0, 0, 0, 0, 0, 0, 0, 0};
    stv[0] = t_start;

    if (__builtin_expect(ii < 16, 1)) {
        const int hk = 2 * xc + (ii >> 3), e = ii & 7;
        const int hf = ii, ef = xc;
        const bool has_mlp = ii < 14;
        const int fb = 14 * xc + ii;
        const int wq7 = w < 7 ? w : 6;
        unsigned tag_lo, tag_hi;
        if (w < 4) {
            __builtin_amdgcn_s_setprio(3);
            const v4u xv = ldgo(x, (lane < 56 ? lane : 0) * 16);
            v4u qa[7];
            const unsigned oq = (224 * (ef & 1) + 56 * w + (lane >> 3)) * 8192 + hf * 512 + (ef >> 1) * 128 + (lane & 7) * 16;
#pragma unroll
            for (int u = 0; u < 7; ++u) qa[u] = ldgo(wq, oq + u * 8 * 8192);
            PIN();
            __builtin_amdgcn_s_barrier();
            PIN();
            STAMP(12);
            float xf[8] = {bflo(xv.x), bfhi(xv.x), bflo(xv.y), bfhi(xv.y), bflo(xv.z), bfhi(xv.z), bflo(xv.w), bfhi(xv.w)};
            float s = ((xf[0] + xf[1]) + (xf[2] + xf[3])) + ((xf[4] + xf[5]) + (xf[6] + xf[7]));
            if (lane >= 56) s = 0.f;
            const float mean = wave_sum(s) * (1.f / 448.f);
            float s2 = 0.f;
#pragma unroll
            for (int j = 0; j < 8; ++j) { float d = xf[j] - mean; s2 += d * d; }
            if (lane >= 56) s2 = 0.f;
            const float var = wave_sum(s2) * (1.f / 448.f);
            const float mb = rbf(mean);
            const float den = rbf(__builtin_amdgcn_sqrtf(rbf(rbf(var) + 1e-5f)));
            const float rden = __builtin_amdgcn_rcpf(den);
            if (lane < 56) {
                f32x4 o0, o1;
#pragma unroll
                for (int j = 0; j < 4; ++j) { o0[j] = rbf(rbf(xf[j] - mb) * rden); o1[j] = rbf(rbf(xf[4 + j] - mb) * rden); }
                *(LAS f32x4*)(L + LW_XN + lane * 32) = o0;
                *(LAS f32x4*)(L + LW_XN + lane * 32 + 16) = o1;
            }
            COMPILER_FENCE();
            float xm[7];
#pragma unroll
            for (int u = 0; u < 7; ++u) xm[u] = *(LAS float*)(L + LW_XN + (224 * (ef & 1) + 56 * w + 8 * u + (lane >> 3)) * 4);
            asm volatile("" : "+s"(epoch));
            const u64 tag = epoch * 6364136223846793005ull + 1442695040888963407ull;
            tag_lo = (unsigned)tag; tag_hi = (unsigned)(tag >> 32);
            PIN();
            STAMP(13);
            float acc[8];
#pragma unroll
            for (int j = 0; j < 8; ++j) acc[j] = 0.f;
#pragma unroll
            for (int u = 0; u < 7; ++u) {
                acc[0] += xm[u] * bflo(qa[u].x); acc[1] += xm[u] * bfhi(qa[u].x); acc[2] += xm[u] * bflo(qa[u].y); acc[3] += xm[u] * bfhi(qa[u].y);
                acc[4] += xm[u] * bflo(qa[u].z); acc[5] += xm[u] * bfhi(qa[u].z); acc[6] += xm[u] * bflo(qa[u].w); acc[7] += xm[u] * bfhi(qa[u].w);
                PIN();
            }
            float r0 = add32(acc[0], acc[4]), r1 = add32(acc[1], acc[5]), r2 = add32(acc[2], acc[6]), r3 = add32(acc[3], acc[7]);
            float s0 = add16(r0, r2), s1 = add16(r1, r3);
            s0 += dpp<DPP_ROR8>(s0); s1 += dpp<DPP_ROR8>(s1);
            if ((lane & 8) == 0) {
                f32x2 o = {s0, s1};
                *(LAS f32x2*)(L + LW_QP + w * 256 + (8 * (lane & 7) + 4 * (lane >> 5) + 2 * ((lane >> 4) & 1)) * 4) = o;
            }
            STAMP(1);
            wait_lgkm();
            if (lane == 0) __hip_atomic_fetch_add((LAS unsigned*)(L + LW_CNT), 1u, __ATOMIC_RELAXED, __HIP_MEMORY_SCOPE_WORKGROUP);
            const unsigned th = (tag_lo >> 8) | (tag_hi << 24);
            if (w == 0) {
                for (int spin = 0; spin < (1 << 20) && *(volatile LAS unsigned*)(L + LW_CNT) < 4u; ++spin) {}
                COMPILER_FENCE();
                f32x2 a = {0.f, 0.f};
#pragma unroll
                for (int v = 0; v < 4; ++v) a += *(LAS f32x2*)(L + LW_QP + v * 256 + (lane & 31) * 8);
                const float ax = a.x, ay = a.y;
                v4u gr = {(f2u(ax) >> 8) | (tag_lo << 24), th, (f2u(ay) >> 8) | (tag_lo << 24), th};
                stsc(rs, lane < 32 ? WS_Q1 + hf * 4096 + ef * 512 + lane * 16 : OOB, gr);
                STAMP(2);
            }
            __builtin_amdgcn_s_sleep(H1_SLEEP);
            {
                const int off = WS_Q1 + hf * 4096 + w * 1024 + lane * 8;
                v2u v0, v1;
                v2u fl = ldsc2(rs, WS_FLAG + (w * 32 + (lane & 31)) * 8);
                for (int spin = 0;; ++spin) {
                    v0 = ldsc2(rs, off); v1 = ldsc2(rs, off + 512);
                    const bool ok = ((v0.x ^ (tag_lo << 24)) | (v1.x ^ (tag_lo << 24))) < 0x1000000u && v0.y == th && v1.y == th;
                    if (__all(ok) || spin > SPIN_CAP) break;
                    COMPILER_FENCE();
                }
                for (int spin = 0; !__all(fl.x == tag_lo && fl.y == tag_hi) && spin < SPIN_CAP; ++spin) {
                    __builtin_amdgcn_s_sleep(8);
                    fl = ldsc2(rs, WS_FLAG + (w * 32 + (lane & 31)) * 8);
                    COMPILER_FENCE();
                }
                const float qv = u2f(v0.x << 8) + u2f(v1.x << 8);
                *(LAS u16*)(L + LW_QV + (w * 64 + lane) * 2) = (u16)pk2(qv, qv);
            }
            STAMP(3);
            __builtin_amdgcn_s_setprio(0);
            bar();
        } else {
            const int kw = w - 4;
            if (kw == 0) { const v2u z = {0u, 0u}; *(LAS v2u*)(L + LW_Z + lane * 8) = z; *(LAS unsigned*)(L + LW_CNT) = 0u; }
            PIN();
            bar();
            v4u wk[16];
            const unsigned ok = (128 * ef + 32 * kw + (n16 >> 2)) * 8192 + hf * 512 + g4 * 64 + (n16 & 3) * 16;
#pragma unroll
            for (int u = 0; u < 16; ++u) wk[u] = ldgo(wuk, ok + (u >> 1) * 4 * 8192 + (u & 1) * 256);
            PIN();
            asm volatile("" : "+s"(epoch));
            const u64 tag = epoch * 6364136223846793005ull + 1442695040888963407ull;
            tag_lo = (unsigned)tag; tag_hi = (unsigned)(tag >> 32);
            STAMP(1);
            bar();
            v4u qf[8];
#pragma unroll
            for (int u = 0; u < 8; ++u) qf[u] = lds128(L + ((n16 >> 2) == (u >> 1) ? LW_QV : LW_Z) + (u & 1) * 256 + g4 * 64 + (n16 & 3) * 16);
            PIN();
            f32x4 d0 = {0.f, 0.f, 0.f, 0.f}, d1 = {0.f, 0.f, 0.f, 0.f};
#pragma unroll
            for (int u = 0; u < 8; ++u) d0 = mfma(wk[u], qf[u], d0);
#pragma unroll
            for (int u = 0; u < 8; ++u) d1 = mfma(wk[8 + u], qf[u], d1);
            float v0 = (lane & 2) ? ((lane & 1) ? d0.w : d0.z) : ((lane & 1) ? d0.y : d0.x);
            float v1 = (lane & 2) ? ((lane & 1) ? d1.w : d1.z) : ((lane & 1) ? d1.y : d1.x);
            v0 += dpp<DPP_X1>(v0); v1 += dpp<DPP_X1>(v1); v0 += dpp<DPP_X2>(v0); v1 += dpp<DPP_X2>(v1);
            const float v = ((lane & 1) ? v1 : v0) * 0.0625f;
            unsigned vb = pk2(v, v) & 0xffffu;
            vb = vb < 0xfffeu ? vb : 0xfffeu;
            if ((lane & 2) == 0) *(LAS u16*)(L + LW_QA + (32 * kw + 16 * (lane & 1) + (n16 & 12) + g4) * 2) = (u16)vb;
            STAMP(3);
        }
        const u64 tag = ((u64)tag_hi << 32) | tag_lo;
        bar();
        if (w == 4) stsc(rs, lane < 16 ? WS_Q2 + hf * 2048 + ef * 256 + lane * 16 : OOB, lds128(L + LW_QA + n16 * 16));
        PIN();
        STAMP(4);
        {
            const unsigned vo = (512 * hk + 64 * w + g4) * 2048 + e * 256;
#pragma unroll
            for (int u = 0; u < 16; ++u) {
                if (u >= COLS_WIN) wait_vm<COLS_WIN - 1>();
                dmas<0>(ckv, vo + u * 8192 + ((n16 ^ ((g4 << 2) | (u & 3))) * 16), LW_COLS + (64 * w + 4 * u) * 256);
            }
        }
        PIN();
        STAMP(5);
        wait_vm<0>();
        bar();
        STAMP(6);
        v4u vf[16];
        {
            const int q = n16 >> 2, p = lane & 3;
#pragma unroll
            for (int t = 0; t < 2; ++t) {
                LAS unsigned char* base = L + LW_COLS + (8 * g4 + 4 * t + q) * 256 + (((2 * w + (p >> 1)) ^ ((q << 2) | ((2 * g4 + t) & 3))) * 16) + 8 * (p & 1);
#pragma unroll
                for (int ks = 0; ks < 16; ++ks) {
                    v2u r = trread(base + ks * 8192);
                    if (t == 0) { vf[ks].x = r.x; vf[ks].y = r.y; } else { vf[ks].z = r.x; vf[ks].w = r.y; }
                }
            }
        }
        PIN();
        const int off3 = WS_S3 + (hk * 32 + (lane >> 2)) * 512 + w * 64 + (lane & 3) * 16;
        if (w == 0) {
            for (int spin = 0; spin < SPIN_CAP; ++spin) {
                const unsigned hv = __builtin_amdgcn_raw_buffer_load_b32(rs, WS_H3 + (hk * 8 + (lane & 7)) * 128, 0, 16);
                if (__all(hv != SENT)) break;
                COMPILER_FENCE();
            }
            if (H3_SLEEP) __builtin_amdgcn_s_sleep(H3_SLEEP);
        }
        STAMP(14);
        bar();
        v4u sc0 = ldsc(rs, off3), sc1 = ldsc(rs, off3 + 16 * 512);
        for (int spin = 0; spin < SPIN_CAP; ++spin) {
            const bool ok0 = __all(umax4(sc0) != SENT), ok1 = __all(umax4(sc1) != SENT);
            if (ok0 && ok1) break;
            if (!ok0) sc0 = ldsc(rs, off3);
            if (!ok1) sc1 = ldsc(rs, off3 + 16 * 512);
            COMPILER_FENCE();
        }
        STAMP(7);
        float sm_m, sm_l;
        {
            float s[16] = {bflo(sc0.x), bfhi(sc0.x), bflo(sc0.y), bfhi(sc0.y), bflo(sc0.z), bfhi(sc0.z), bflo(sc0.w), bfhi(sc0.w),
                           bflo(sc1.x), bfhi(sc1.x), bflo(sc1.y), bfhi(sc1.y), bflo(sc1.z), bfhi(sc1.z), bflo(sc1.w), bfhi(sc1.w)};
            float m = s[0];
#pragma unroll
            for (int j = 1; j < 16; ++j) m = fmaxf(m, s[j]);
            m = fmaxf(m, dpp<DPP_X1>(m)); m = fmaxf(m, dpp<DPP_ROR4>(m)); m = fmaxf(m, dpp<DPP_ROR8>(m));
            m = max16(m); m = max32(m);
            const float mm = -m * L2E;
#pragma unroll
            for (int j = 0; j < 16; ++j) s[j] = __builtin_amdgcn_exp2f(__builtin_fmaf(s[j], L2E, mm));
            v4u p0 = {pk2(s[0], s[1]), pk2(s[2], s[3]), pk2(s[4], s[5]), pk2(s[6], s[7])};
            v4u p1 = {pk2(s[8], s[9]), pk2(s[10], s[11]), pk2(s[12], s[13]), pk2(s[14], s[15])};
            const int tile = lane >> 2, head = 2 * w + ((lane >> 1) & 1), gp = 2 * (tile & 1) + (lane & 1);
            LAS unsigned char* dst = L + LW_PF + (tile >> 1) * 1024 + head * 64 + ((gp ^ ((-(head >> 2)) & 3)) * 16);
            *(LAS v4u*)dst = p0;
            *(LAS v4u*)(dst + 8192) = p1;
            PIN();
            sm_m = m;
            sm_l = (((s[0] + s[1]) + (s[2] + s[3])) + ((s[4] + s[5]) + (s[6] + s[7]))) + (((s[8] + s[9]) + (s[10] + s[11])) + ((s[12] + s[13]) + (s[14] + s[15])));
        }
        bar();
        v4u m1[2], m2[2];
        unsigned b1v = 0;
        {
            v4u pf[16];
            LAS unsigned char* pb = L + LW_PF + n16 * 64 + ((g4 ^ ((-(n16 >> 2)) & 3)) * 16);
#pragma unroll
            for (int ks = 0; ks < 16; ++ks) pf[ks] = lds128(pb + ks * 1024);
            PIN();
            f32x4 d = {0.f, 0.f, 0.f, 0.f};
#pragma unroll
            for (int ks = 0; ks < 16; ++ks) d = mfma(vf[ks], pf[ks], d);
            float l = sm_l;
            l += dpp<DPP_X1>(l); l += dpp<DPP_ROR4>(l); l += dpp<DPP_ROR8>(l); l = add16(l, l); l = add32(l, l);
            if ((lane & 61) == 0) { v2u o = {nosent(f2u(sm_m)), nosent(f2u(l))}; *(LAS v2u*)(L + LW_ST + (2 * w + (lane >> 1)) * 8) = o; }
            v2u o = {nosent(pk2(d.x, d.y)), nosent(pk2(d.z, d.w))};
            *(LAS v2u*)(L + LW_TR + n16 * 272 + w * 32 + g4 * 8) = o;
        }
        bar();
        if (w < 4) stsc(rs, WS_P4 + ((4 * w + g4) * 16 + hk) * 2048 + e * 256 + n16 * 16, lds128(L + LW_TR + (4 * w + g4) * 272 + n16 * 16));
        else if (w == 4 && e == 0) { if (lane < 8) stsc(rs, WS_ST4 + hk * 128 + lane * 16, lds128(L + LW_ST + lane * 16)); }
        PIN();
        STAMP(8);
        if (has_mlp) {
            {
                const char* p1 = (const char*)w1 + (size_t)(64 * wq7 + (lane >> 1)) * 3584 + fb * 32 + (lane & 1) * 16;
                m1[0] = ldg(p1); m1[1] = ldg(p1 + 32 * 3584);
                const char* p2 = (const char*)w2 + (size_t)(fb * 16 + 2 * w) * 896 + (lane < 56 ? lane : 0) * 16;
                m2[0] = ldg(p2); m2[1] = ldg(p2 + 896);
                b1v = *(const GAS unsigned*)((const char*)b1 + fb * 32 + w * 4);
            }
            PIN();
            if (w < 7) {
                float acc[8];
#pragma unroll
                for (int j = 0; j < 8; ++j) acc[j] = 0.f;
#pragma unroll
                for (int u = 0; u < 2; ++u) {
                    const float xm = *(LAS float*)(L + LW_XN + (64 * w + 32 * u + (lane >> 1)) * 4);
                    acc[0] += xm * bflo(m1[u].x); acc[1] += xm * bfhi(m1[u].x); acc[2] += xm * bflo(m1[u].y); acc[3] += xm * bfhi(m1[u].y);
                    acc[4] += xm * bflo(m1[u].z); acc[5] += xm * bfhi(m1[u].z); acc[6] += xm * bflo(m1[u].w); acc[7] += xm * bfhi(m1[u].w);
                }
                float r0 = add32(acc[0], acc[4]), r1 = add32(acc[1], acc[5]), r2 = add32(acc[2], acc[6]), r3 = add32(acc[3], acc[7]);
                float s0 = add16(r0, r2), s1 = add16(r1, r3);
                s0 += dpp<DPP_X2>(s0); s1 += dpp<DPP_X2>(s1);
                s0 += dpp<DPP_ROR4>(s0); s1 += dpp<DPP_ROR4>(s1); s0 += dpp<DPP_ROR8>(s0); s1 += dpp<DPP_ROR8>(s1);
                if ((lane & 14) == 0) {
                    f32x2 o = {s0, s1};
                    *(LAS f32x2*)(L + LW_HP + w * 64 + (8 * (lane & 1) + 4 * (lane >> 5) + 2 * ((lane >> 4) & 1)) * 4) = o;
                }
            }
            bar();
            f32x2 hh = {0.f, 0.f};
#pragma unroll
            for (int v = 0; v < 7; ++v) hh += *(LAS f32x2*)(L + LW_HP + v * 64 + w * 8);
            float h0 = rbf(rbf(hh.x) + bflo(b1v)), h1 = rbf(rbf(hh.y) + bfhi(b1v));
            h0 = h0 > 0.f ? h0 : 0.f; h1 = h1 > 0.f ? h1 : 0.f;
            h0 = rbf(h0 * h0); h1 = rbf(h1 * h1);
            if (lane < 56) {
                f32x4 o0 = {h0 * bflo(m2[0].x) + h1 * bflo(m2[1].x), h0 * bfhi(m2[0].x) + h1 * bfhi(m2[1].x),
                            h0 * bflo(m2[0].y) + h1 * bflo(m2[1].y), h0 * bfhi(m2[0].y) + h1 * bfhi(m2[1].y)};
                f32x4 o1 = {h0 * bflo(m2[0].z) + h1 * bflo(m2[1].z), h0 * bfhi(m2[0].z) + h1 * bfhi(m2[1].z),
                            h0 * bflo(m2[0].w) + h1 * bflo(m2[1].w), h0 * bfhi(m2[0].w) + h1 * bfhi(m2[1].w)};
                *(LAS f32x4*)(L + LW_COLS + w * 1792 + lane * 32) = o0;
                *(LAS f32x4*)(L + LW_COLS + w * 1792 + lane * 32 + 16) = o1;
            }
            bar();
            if (tid < 112) {
                f32x4 a = {0.f, 0.f, 0.f, 0.f};
#pragma unroll
                for (int v = 0; v < 8; ++v) a += *(LAS f32x4*)(L + LW_COLS + v * 1792 + tid * 16);
                float a0 = a.x, a1 = a.y, a2 = a.z, a3 = a.w;
                v4u o = {nosent(f2u(a0)), nosent(f2u(a1)), nosent(f2u(a2)), nosent(f2u(a3))};
                stsc(rs, WS_M5 + (tid >> 3) * (112 * 128) + fb * 128 + (tid & 7) * 16, o);
            }
            STAMP(9);
        } else {
            const int fi = 2 * xc + (ii - 14);
            if (fi < 14) {
                const unsigned xb = *(const GAS u16*)(x + fi * 32 + (lane & 31)), b2b = *(const GAS u16*)(b2 + fi * 32 + (lane & 31));
                const int offm = WS_M5 + fi * (112 * 128) + wq7 * 2048 + lane * 16, offs = WS_P5 + fi * 16384 + w * 2048 + lane * 16;
                v4u a0 = ldsc(rs, offm), a1 = ldsc(rs, offm + 1024), c0 = ldsc(rs, offs), c1 = ldsc(rs, offs + 1024);
                for (int spin = 0; spin < SPIN_CAP; ++spin) {
                    const bool k0 = __all(umax4(a0) != SENT), k1 = __all(umax4(a1) != SENT), k2 = __all(umax4(c0) != SENT), k3 = __all(umax4(c1) != SENT);
                    if (k0 && k1 && k2 && k3) break;
                    if (!k0) a0 = ldsc(rs, offm);
                    if (!k1) a1 = ldsc(rs, offm + 1024);
                    if (!k2) c0 = ldsc(rs, offs);
                    if (!k3) c1 = ldsc(rs, offs + 1024);
                    COMPILER_FENCE();
                }
                STAMP(10);
                {
                    const f32x4 am = __builtin_bit_cast(f32x4, a0) + __builtin_bit_cast(f32x4, a1);
                    const f32x4 as = __builtin_bit_cast(f32x4, c0) + __builtin_bit_cast(f32x4, c1);
                    float s0 = add16(add32(am.x, am.z), add32(am.y, am.w)), s1 = add16(add32(as.x, as.z), add32(as.y, as.w));
                    s0 += dpp<DPP_ROR8>(s0); s1 += dpp<DPP_ROR8>(s1);
                    f32x2 o = {s0, s1};
                    if ((lane & 8) == 0) *(LAS f32x2*)(L + LW_FR + w * 256 + ((lane & 7) * 4 + 2 * (lane >> 5) + ((lane >> 4) & 1)) * 8) = o;
                }
                bar();
                if (tid < 32) {
                    f32x2 a = {0.f, 0.f};
#pragma unroll
                    for (int v = 0; v < 7; ++v) a += *(LAS f32x2*)(L + LW_FR + v * 256 + tid * 8);
                    const float sv = a.y + *(LAS float*)(L + LW_FR + 7 * 256 + tid * 8 + 4);
                    const float o2 = rbf(rbf(a.x) + bflo(b2b));
                    const float gate = rbf(o2 * __builtin_amdgcn_rcpf(1.0f + __builtin_amdgcn_exp2f(-o2 * L2E)));
                    const float o = bflo(xb) + rbf(rbf(sv) * gate);
                    *(GAS u16*)(out + fi * 32 + tid) = (u16)pk2(o, o);
                    if (fi == 0 && tid == 0) *(GAS u64*)(ws + WS_E) = tag;
                }
                STAMP(11);
            }
        }
    } else {
        const int ia = ii - 16, ai = xc * 16 + ia;
        const int hd = 2 * xc + (ia >> 3), ch = (ia >> 2) & 1, dq = ia & 3;
        if (w < 4) {
            v4u a[32], q[16];
            const int r8 = lane >> 3, pos = lane & 7;
            const unsigned vo0 = (64 * ai + 16 * w + r8) * 2048 + ((pos ^ (r8 >> 1)) * 16);
            const unsigned vo1 = (64 * ai + 16 * w + 8 + r8) * 2048 + ((pos ^ (4 + (r8 >> 1))) * 16);
            const int ring = LA_RING + w * RS * 2048;
            LAS unsigned char* ra0 = L + ring + n16 * 128 + ((g4 ^ (n16 >> 1)) * 16);
            LAS unsigned char* ra1 = L + ring + n16 * 128 + (((4 + g4) ^ (n16 >> 1)) * 16);
            LAS unsigned char* qb = L + LA_Q + n16 * 64 + ((g4 ^ ((-(n16 >> 2)) & 3)) * 16);
            f32x4 d = {0.f, 0.f, 0.f, 0.f};
            static_for<0, 16>([&](auto I) {
                constexpr int i = I.value;
                static_for<n_issued(i - 1), n_issued(i)>([&](auto J) {
                    constexpr int j = J.value;
                    dmas<j * 128>(ckv, vo0, ring + (j % RS) * 2048 - j * 128);
                    dmas<j * 128>(ckv, vo1, ring + (j % RS) * 2048 + 1024 - j * 128);
                });
                if constexpr (n_issued(i) == 16 && n_issued(i - 1) < 16) {
                    STAMP(1);
                    bar();
                    STAMP(2);
#pragma unroll
                    for (int j = 0; j < 16; ++j) q[j] = lds128(qb + j * 1024);
                    PIN();
#pragma unroll
                    for (int j = 0; j < 16; ++j) d = mfma(a[j], q[j], d);
                    PIN();
#pragma unroll
                    for (int j = 0; j < 16; ++j) q[j] = lds128(qb + (16 + j) * 1024);
                    PIN();
#pragma unroll
                    for (int j = 16; j < 2 * i; ++j) d = mfma(a[j], q[j - 16], d);
                    PIN();
                }
                wait_vm<2 * (n_issued(i) - i - 1)>();
                a[2 * i] = lds128(ra0 + (i % RS) * 2048);
                a[2 * i + 1] = lds128(ra1 + (i % RS) * 2048);
                if constexpr (n_issued(i) < 16) {
                    asm volatile("s_waitcnt lgkmcnt(0)" : "+v"(a[2 * i]), "+v"(a[2 * i + 1]) :: "memory");
                } else {
                    d = mfma(a[2 * i], q[2 * i - 16], d);
                    d = mfma(a[2 * i + 1], q[2 * i + 1 - 16], d);
                    PIN();
                }
            });
            unsigned a0 = nosent(pk2(d.x, d.y)), a1 = nosent(pk2(d.z, d.w)), c0 = a0, c1 = a1;
            swap16(a0, c0); swap16(a1, c1);
            v4u o = {a0, a1, c0, c1};
            stsc(rs, (g4 & 1) == 0 ? WS_S3 + (ai * 4 + w) * 512 + n16 * 32 + (g4 >> 1) * 16 : OOB, o);
            PIN();
            STAMP(3);
        } else {
            const int tw = w - 4;
            if (tw == 0) {
#pragma unroll
                for (int u = 0; u < 10; ++u) stsc(rs, WS_Q2 + ai * 10240 + u * 1024 + lane * 16, SV);
            }
            PIN();
            asm volatile("" : "+s"(epoch));
            const u64 tag = epoch * 6364136223846793005ull + 1442695040888963407ull;
            const unsigned tag_lo = (unsigned)tag, tag_hi = (unsigned)(tag >> 32);
            if (tw == 0) {
                wait_vm<0>();
                v2u t = {tag_lo, tag_hi};
                stsc2(rs, lane == 0 ? WS_FLAG + ai * 8 : OOB, t);
            }
            STAMP(1);
            while (__builtin_amdgcn_s_memrealtime() - t_start < FLAG_TICKS) __builtin_amdgcn_s_sleep(2);
            for (int spin = 0;; ++spin) {
                v4u v = ldsc(rs, WS_FLAG + lane * 16);
                const bool ok = v.x == tag_lo && v.y == tag_hi && v.z == tag_lo && v.w == tag_hi;
                if (__all(ok) || spin > SPIN_CAP) break;
                __builtin_amdgcn_s_sleep(8);
                COMPILER_FENCE();
            }
            STAMP(2);
            while (__builtin_amdgcn_s_memrealtime() - t_start < H2_TICKS) __builtin_amdgcn_s_sleep(1);
            for (int spin = 0; spin < SPIN_CAP; ++spin) {
                v4u v = ldsc(rs, WS_Q2 + tw * 8192 + (ia & 7) * 1024 + lane * 16);
                if (__all(umax4(v) != SENT)) break;
                COMPILER_FENCE();
            }
            STAMP(12);
            {
                v4u qv[8];
                const int off = WS_Q2 + tw * 8192 + lane * 16;
#pragma unroll
                for (int u = 0; u < 8; ++u) qv[u] = ldsc(rs, off + u * 1024);
                for (int spin = 0; spin < SPIN_CAP; ++spin) {
                    bool all = true;
#pragma unroll
                    for (int u = 0; u < 8; ++u) {
                        if (!__all(umax4(qv[u]) != SENT)) { all = false; qv[u] = ldsc(rs, off + u * 1024); }
                    }
                    if (all) break;
                    COMPILER_FENCE();
                }
                LAS unsigned char* dst = L + LA_Q + (lane >> 2) * 1024 + tw * 256 + (((lane & 3) ^ ((-tw) & 3)) * 16);
#pragma unroll
                for (int u = 0; u < 8; ++u) *(LAS v4u*)(dst + (u & 1) * 16384 + (u >> 1) * 64) = qv[u];
            }
            STAMP(3);
            bar();
            if (tw == 0) __builtin_amdgcn_raw_buffer_store_b32(0u, rs, lane == 0 ? WS_H3 + ai * 128 : OOB, 0, 16);
            {
                const int r8 = lane >> 3, pos = lane & 7;
                const unsigned vo = (512 * ch + 128 * tw + r8) * 8192 + hd * 512 + dq * 128;
#pragma unroll
                for (int u = 0; u < TAIL_X; ++u)
                    dmas<0>(wuv, vo + u * 65536 + ((pos ^ ((((r8 >> 1) & 1) | ((u & 1) << 1)) << 1)) * 16), LA_UV + (128 * tw + 8 * u) * 128);
            }
            STAMP(4);
        }
        bar();
        {
            const int r8 = lane >> 3, pos = lane & 7, tq = w & 3, th = w >> 2;
            const unsigned vo = (512 * ch + 128 * tq + r8) * 8192 + hd * 512 + dq * 128;
#pragma unroll
            for (int v = 0; v < (16 - TAIL_X) / 2; ++v) {
                const int u = TAIL_X + 2 * v + th;
                dmas<0>(wuv, vo + u * 65536 + ((pos ^ ((((r8 >> 1) & 1) | ((u & 1) << 1)) << 1)) * 16), LA_UV + (128 * tq + 8 * u) * 128);
            }
            const unsigned so = (hd * 256 + dq * 64 + 8 * w) * 896 + lane * 16;
            if (lane < 56) {
#pragma unroll
                for (int u = 0; u < 8; ++u) dmas<0>(wo, so + u * 896, LA_WO + (8 * w + u) * 928 + w * 128);
            }
        }
        PIN();
        wait_vm<0>();
        STAMP(5);
        bar();
        const int off4 = WS_P4 + (hd * 16 + (lane >> 3)) * 2048 + ch * 1024 + w * 128 + (lane & 7) * 16;
        const int soff = WS_ST4 + g4 * 512 + hd * 8;
        v2u s0 = ldsc2(rs, soff), s1 = ldsc2(rs, soff + 128), s2 = ldsc2(rs, soff + 256), s3 = ldsc2(rs, soff + 384);
        v4u c0 = ldsc(rs, off4), c1 = ldsc(rs, off4 + 8 * 2048);
        PIN();
        v4u uf[8], of[8];
        {
            const int q = n16 >> 2, p = lane & 3;
#pragma unroll
            for (int t = 0; t < 2; ++t) {
                const int sw = ((q >> 1) | ((g4 & 1) << 1)) << 1;
                LAS unsigned char* ub = L + LA_UV + (64 * w + 8 * g4 + 4 * t + q) * 128 + 8 * (p & 1);
                const int wq7 = w < 7 ? w : 6;
                LAS unsigned char* ob = L + LA_WO + (8 * g4 + 4 * t + q) * 928 + g4 * 128 + wq7 * 128 + p * 8;
#pragma unroll
                for (int ks = 0; ks < 2; ++ks)
#pragma unroll
                    for (int ct = 0; ct < 4; ++ct) {
                        v2u r = trread(ub + ks * 4096 + (((2 * ct + (p >> 1)) ^ sw) * 16));
                        v2u o = trread(ob + ks * (32 * 928 + 512) + ct * 32);
                        if (t == 0) { uf[ks * 4 + ct].x = r.x; uf[ks * 4 + ct].y = r.y; of[ks * 4 + ct].x = o.x; of[ks * 4 + ct].y = o.y; }
                        else { uf[ks * 4 + ct].z = r.x; uf[ks * 4 + ct].w = r.y; of[ks * 4 + ct].z = o.x; of[ks * 4 + ct].w = o.y; }
                    }
            }
        }
        PIN();
        STAMP(6);
        {
            for (int spin = 0; spin < SPIN_CAP; ++spin) {
                const bool ks = __all(max(max(max(s0.x, s0.y), max(s1.x, s1.y)), max(max(s2.x, s2.y), max(s3.x, s3.y))) != SENT);
                const bool kc = __all(max(umax4(c0), umax4(c1)) != SENT);
                if (ks && kc) break;
                if (!ks) { s0 = ldsc2(rs, soff); s1 = ldsc2(rs, soff + 128); s2 = ldsc2(rs, soff + 256); s3 = ldsc2(rs, soff + 384); }
                if (!kc) { c0 = ldsc(rs, off4); c1 = ldsc(rs, off4 + 8 * 2048); }
                COMPILER_FENCE();
            }
            STAMP(7);
            {
                LAS unsigned char* bn = L + LA_BN + w * 2304;
                *(LAS v4u*)(bn + (lane >> 3) * 144 + (lane & 7) * 16) = c0;
                *(LAS v4u*)(bn + (8 + (lane >> 3)) * 144 + (lane & 7) * 16) = c1;
                COMPILER_FENCE();
                c0 = lds128(bn + n16 * 144 + g4 * 16);
                c1 = lds128(bn + n16 * 144 + 64 + g4 * 16);
            }
            f32x4 d[4];
#pragma unroll
            for (int ct = 0; ct < 4; ++ct) {
                f32x4 z = {0.f, 0.f, 0.f, 0.f};
                d[ct] = mfma(c0, uf[ct], z);
            }
#pragma unroll
            for (int ct = 0; ct < 4; ++ct) d[ct] = mfma(c1, uf[4 + ct], d[ct]);
            const float m0 = u2f(s0.x), m1 = u2f(s1.x), m2 = u2f(s2.x), m3 = u2f(s3.x);
            float M = fmaxf(fmaxf(m0, m1), fmaxf(m2, m3));
            M = max16(M); M = max32(M);
            float f0 = __builtin_amdgcn_exp2f((m0 - M) * L2E), f1 = __builtin_amdgcn_exp2f((m1 - M) * L2E);
            float f2 = __builtin_amdgcn_exp2f((m2 - M) * L2E), f3 = __builtin_amdgcn_exp2f((m3 - M) * L2E);
            float Ls = (f0 * u2f(s0.y) + f1 * u2f(s1.y)) + (f2 * u2f(s2.y) + f3 * u2f(s3.y));
            Ls = add16(Ls, Ls); Ls = add32(Ls, Ls);
            const float rl = __builtin_amdgcn_rcpf(Ls);
            f0 *= rl; f1 *= rl; f2 *= rl; f3 *= rl;
            float cx[4];
#pragma unroll
            for (int ct = 0; ct < 4; ++ct) cx[ct] = (f0 * d[ct].x + f1 * d[ct].y) + (f2 * d[ct].z + f3 * d[ct].w);
            const float r0 = add32(cx[0], cx[2]), r1 = add32(cx[1], cx[3]);
            const float cv = add16(r0, r1);
            const float hi = rbf(cv), lo = cv - hi;
            const unsigned pk = pk2(hi, lo);
            *(LAS u16*)(L + LA_CT + w * 144 + lane * 2) = (u16)pk;
            *(LAS u16*)(L + LA_CT + (8 + w) * 144 + lane * 2) = (u16)(pk >> 16);
        }
        bar();
        if (w < 7) {
            const v4u a0 = lds128(L + LA_CT + n16 * 144 + g4 * 16), a1 = lds128(L + LA_CT + n16 * 144 + 64 + g4 * 16);
            f32x4 d[4];
#pragma unroll
            for (int ct = 0; ct < 4; ++ct) {
                f32x4 z = {0.f, 0.f, 0.f, 0.f};
                d[ct] = mfma(a0, of[ct], z);
            }
#pragma unroll
            for (int ct = 0; ct < 4; ++ct) d[ct] = mfma(a1, of[4 + ct], d[ct]);
            float sx[4];
#pragma unroll
            for (int ct = 0; ct < 4; ++ct) sx[ct] = (d[ct].x + d[ct].y) + (d[ct].z + d[ct].w);
            const float r0 = add32(sx[0], sx[2]), r1 = add32(sx[1], sx[3]);
            const float sv = add16(r0, r1);
            const unsigned su = nosent(f2u(sv));
            v4u o;
            o.x = su;
            o.y = (unsigned)__builtin_amdgcn_update_dpp(0, (int)su, 0x55, 0xf, 0xf, true);
            o.z = (unsigned)__builtin_amdgcn_update_dpp(0, (int)su, 0xAA, 0xf, 0xf, true);
            o.w = (unsigned)__builtin_amdgcn_update_dpp(0, (int)su, 0xFF, 0xf, 0xf, true);
            stsc(rs, (lane & 3) == 0 ? WS_P5 + (2 * w + (lane >> 5)) * 16384 + ai * 128 + (lane & 31) * 4 : OOB, o);
        }
        STAMP(8);
    }
    STAMP_OUT();
}

#include <cstdio>
static void dump_stamps(void* d_ws);
extern "C" void kernel_launch(void* const* d_in, const int* in_sizes, int n_in, void* d_out, int out_size, void* d_ws, size_t ws_size,
                              hipStream_t stream) {
    (void)hipFuncSetAttribute((const void*)mla_decode_kernel, hipFuncAttributeMaxDynamicSharedMemorySize, LDS_BYTES);
    mla_decode_kernel<<<256, 512, LDS_BYTES, stream>>>((const u16*)d_in[0], (const u16*)d_in[1], (const u16*)d_in[2], (const u16*)d_in[3],
                                                       (const u16*)d_in[4], (const u16*)d_in[5], (const u16*)d_in[7], (const u16*)d_in[8],
                                                       (const u16*)d_in[9], (const u16*)d_in[10], (u16*)d_out, (unsigned char*)d_ws);
    hipStreamCaptureStatus cs; (void)hipStreamIsCapturing(stream, &cs);
    if (cs == hipStreamCaptureStatusNone) {
        (void)hipStreamSynchronize(stream);
        for (int i = 0; i < n_in; ++i) { hipPointerAttribute_t at; (void)hipPointerGetAttributes(&at, d_in[i]); fprintf(stderr, "in%d %p type %d managed %d flags %u\n", i, d_in[i], (int)at.type, at.isManaged, at.allocationFlags); }
        { hipPointerAttribute_t at; (void)hipPointerGetAttributes(&at, d_ws); fprintf(stderr, "ws %p type %d managed %d flags %u out %p\n", d_ws, (int)at.type, at.isManaged, at.allocationFlags, d_out); }
        dump_stamps(d_ws);
        for (int r = 0; r < 3; ++r) {
            mla_decode_kernel<<<256, 512, LDS_BYTES, stream>>>((const u16*)d_in[0], (const u16*)d_in[1], (const u16*)d_in[2], (const u16*)d_in[3],
                                                       (const u16*)d_in[4], (const u16*)d_in[5], (const u16*)d_in[7], (const u16*)d_in[8],
                                                       (const u16*)d_in[9], (const u16*)d_in[10], (u16*)d_out, (unsigned char*)d_ws);
        }
        (void)hipStreamSynchronize(stream);
        fprintf(stderr, "--- warm\n");
        dump_stamps(d_ws);
    }
}

#include <cstdio>
#include <vector>
#include <algorithm>
static void dump_stamps(void* d_ws) {
    std::vector<u64> h(256 * 8 * NST);
    (void)hipMemcpy(h.data(), (char*)d_ws + STAMP_OFF, h.size() * 8, hipMemcpyDeviceToHost);
    u64 t0 = ~0ull;
    for (int i = 0; i < 256 * 8; ++i) t0 = std::min(t0, h[i * NST]);
    auto show = [&](const char* name, int blo, int bhi, int wlo, int whi, int s, int finals) {
        std::vector<double> v;
        for (int b = blo; b < bhi; ++b) {
            const int ii = b >> 3, xc = b & 7;
            if (finals == 1 && !(ii >= 14 && ii < 16 && 2 * xc + ii - 14 < 14)) continue;
            if (finals == 2 && ii >= 14) continue;
            for (int w = wlo; w < whi; ++w) v.push_back((double)(long long)(h[(b * 8 + w) * NST + s] - t0) * 0.01);
        }
        std::sort(v.begin(), v.end());
        fprintf(stderr, "  %-22s min %.2f p10 %.2f med %.2f p90 %.2f max %.2f\n", name, v[0], v[v.size() / 10], v[v.size() / 2], v[v.size() * 9 / 10], v.back());
    };
    show("W start", 0, 128, 0, 8, 0, 0);
    show("W.Q wq issued (B0)", 0, 128, 0, 4, 12, 0);
    show("W.Q LN done", 0, 128, 0, 4, 13, 0);
    show("W.Q q partial done", 0, 128, 0, 4, 1, 0);
    show("W q published", 0, 128, 0, 1, 2, 0);
    show("W.Q hop1 done", 0, 128, 0, 4, 3, 0);
    show("W.K wuk issued", 0, 128, 4, 8, 1, 0);
    show("W.K qabs computed", 0, 128, 4, 8, 3, 0);
    show("W qabs published", 0, 128, 0, 8, 4, 0);
    show("W cols issued", 0, 128, 0, 8, 5, 0);
    show("W cols landed", 0, 128, 0, 8, 6, 0);
    show("W hint3 seen", 0, 128, 0, 1, 14, 0);
    show("W hop3 done", 0, 128, 0, 8, 7, 0);
    show("W PV published", 0, 128, 0, 8, 8, 0);
    show("W MLP published", 0, 128, 0, 8, 9, 2);
    show("F hop5 done", 0, 128, 0, 8, 10, 1);
    show("F out stored", 0, 128, 0, 8, 11, 1);
    show("A start", 128, 256, 0, 8, 0, 0);
    show("A.S rows issued", 128, 256, 0, 4, 1, 0);
    show("A.S qabs ready", 128, 256, 0, 4, 2, 0);
    show("A.S scores published", 128, 256, 0, 4, 3, 0);
    show("A.T flag out", 128, 256, 4, 5, 1, 0);
    show("A.T flags seen", 128, 256, 4, 8, 2, 0);
    show("A.T hop2 sample ok", 128, 256, 4, 8, 12, 0);
    show("A.T hop2 done", 128, 256, 4, 8, 3, 0);
    show("A.T tail X issued", 128, 256, 4, 8, 4, 0);
    show("A tail landed", 128, 256, 0, 8, 5, 0);
    show("A frags read", 128, 256, 0, 8, 6, 0);
    show("A hop4 done", 128, 256, 0, 8, 7, 0);
    show("A out published", 128, 256, 0, 8, 8, 0);
}
```

```cpp
#include <hip/hip_runtime.h>
#include <hip/hip_bf16.h>

typedef unsigned short u16;
typedef unsigned long long u64;
typedef unsigned v4u __attribute__((ext_vector_type(4)));
typedef unsigned v2u __attribute__((ext_vector_type(2)));
typedef float f32x4 __attribute__((ext_vector_type(4)));
typedef float f32x2 __attribute__((ext_vector_type(2)));
typedef __bf16 bf16x8 __attribute__((ext_vector_type(8)));
typedef __bf16 bf16x2 __attribute__((ext_vector_type(2)));
typedef short s16x4 __attribute__((ext_vector_type(4)));
#define LAS __attribute__((address_space(3)))
#define GAS __attribute__((address_space(1)))
#define DEV __device__ __forceinline__
typedef __amdgpu_buffer_rsrc_t rsrc_t;

constexpr int WS_E = 0;
constexpr int WS_FLAG = 4096;
constexpr int WS_Q1 = 8192;
constexpr int WS_Q2 = WS_Q1 + 65536;
constexpr int WS_S3 = WS_Q2 + 32768;
constexpr int WS_P4 = WS_S3 + 262144;
constexpr int WS_ST4 = WS_P4 + 524288;
constexpr int WS_P5 = WS_ST4 + 4096;
constexpr int WS_M5 = WS_P5 + 14 * 16384;
constexpr int WS_H3 = WS_M5 + 14 * 112 * 128;
constexpr int WS_END = WS_Q2 + 128 * 10240;
static_assert(WS_H3 + 128 * 128 <= WS_END, "workspace map");
constexpr unsigned SENT = 0xffffffffu;
constexpr int SPIN_CAP = 1 << 14;
constexpr int OOB = 0x7ffffff0;
constexpr unsigned CODE_LINES = 145;

constexpr int LW_COLS = 0;
constexpr int LW_PF = 131072;
constexpr int LW_XN = LW_PF + 16384;
constexpr int LW_QP = LW_XN + 2048;
constexpr int LW_QV = LW_QP + 1024;
constexpr int LW_HP = LW_QV + 512;
constexpr int LW_FR = LW_HP + 512;
constexpr int LW_TR = LW_FR + 2048;
constexpr int LW_ST = LW_TR + 16 * 272;
constexpr int LW_QA = LW_ST + 128;
constexpr int LW_Z = LW_QA + 256;
constexpr int LW_CNT = LW_Z + 512;
#define RS 4
constexpr int LA_Q = 0;
constexpr int LA_RING = 32768;
constexpr int LA_WO = 0;
constexpr int LA_BN = LA_WO + 60416;
constexpr int LA_CT = LA_BN + 8 * 2304;
constexpr int LA_UV = 83968; static_assert(LA_RING + 4 * RS * 2048 <= 83968, "ring");
static_assert(LA_CT + 2304 <= LA_UV && LA_UV + 65536 <= 161280 - 256, "A LDS map");
constexpr int LDS_BYTES = 161280;

#define A_W0 1
#define A_SLOWN 2
#define EARLY_COLS 0
#define H1_SLEEP 8
#define H3_SLEEP 0
#define TAIL_X 4
#define COLS_WIN 16
#define H2_TICKS 280
#define FLAG_TICKS 150
#define STAMP(i) do {} while (0)
#define STAMP_OUT() do {} while (0)

DEV unsigned f2u(float f) { return __builtin_bit_cast(unsigned, f); }
DEV float u2f(unsigned u) { return __builtin_bit_cast(float, u); }
DEV float bflo(unsigned w) { return u2f(w << 16); }
DEV float bfhi(unsigned w) { return u2f(w & 0xffff0000u); }
DEV unsigned pk2(float a, float b) { f32x2 p = {a, b}; bf16x2 r = __builtin_convertvector(p, bf16x2); return __builtin_bit_cast(unsigned, r); }
DEV float rbf(float a) { return bflo(pk2(a, a)); }
DEV unsigned nosent(unsigned u) { return u < 0xfffffffeu ? u : 0xfffffffeu; }
DEV f32x4 mfma(v4u a, v4u b, f32x4 c) {
    return __builtin_amdgcn_mfma_f32_16x16x32_bf16(__builtin_bit_cast(bf16x8, a), __builtin_bit_cast(bf16x8, b), c, 0, 0, 0);
}
template <int CTRL> DEV float dpp(float v) { return u2f((unsigned)__builtin_amdgcn_update_dpp(0, (int)f2u(v), CTRL, 0xf, 0xf, true)); }
constexpr int DPP_X1 = 0xB1, DPP_X2 = 0x4E, DPP_ROR4 = 0x124, DPP_ROR8 = 0x128;
DEV void swap16(unsigned& a, unsigned& b) { auto r = __builtin_amdgcn_permlane16_swap(a, b, false, false); a = r[0]; b = r[1]; }
DEV void swap32(unsigned& a, unsigned& b) { auto r = __builtin_amdgcn_permlane32_swap(a, b, false, false); a = r[0]; b = r[1]; }
DEV float add16(float a, float b) { unsigned x = f2u(a), y = f2u(b); swap16(x, y); return u2f(x) + u2f(y); }
DEV float add32(float a, float b) { unsigned x = f2u(a), y = f2u(b); swap32(x, y); return u2f(x) + u2f(y); }
DEV float max16(float a) { unsigned x = f2u(a), y = x; swap16(x, y); return fmaxf(u2f(x), u2f(y)); }
DEV float max32(float a) { unsigned x = f2u(a), y = x; swap32(x, y); return fmaxf(u2f(x), u2f(y)); }
DEV float wave_sum(float v) {
    v += dpp<DPP_X1>(v); v += dpp<DPP_X2>(v); v += dpp<DPP_ROR4>(v); v += dpp<DPP_ROR8>(v);
    v = add16(v, v); v = add32(v, v); return v;
}
DEV v4u ldsc(rsrc_t rs, int off) { return __builtin_amdgcn_raw_buffer_load_b128(rs, off, 0, 16); }
DEV v2u ldsc2(rsrc_t rs, int off) { return __builtin_amdgcn_raw_buffer_load_b64(rs, off, 0, 16); }
DEV void stsc(rsrc_t rs, int off, v4u v) { __builtin_amdgcn_raw_buffer_store_b128(v, rs, off, 0, 16); }
DEV void stsc2(rsrc_t rs, int off, v2u v) { __builtin_amdgcn_raw_buffer_store_b64(v, rs, off, 0, 16); }
DEV unsigned umax4(v4u v) { return max(max(v.x, v.y), max(v.z, v.w)); }
DEV v4u ldg(const void* p) { return __builtin_nontemporal_load((const GAS v4u*)p); }
template <int IMM> DEV void dmas(const void* base, unsigned voff, int ldst) {
    asm volatile("s_mov_b32 m0, %2\n\ts_nop 0\n\tglobal_load_lds_dwordx4 %0, %1 offset:%3 nt" :: "v"(voff), "s"(base), "s"(ldst), "i"(IMM) : "memory");
}
template <int N> DEV void wait_vm() { asm volatile("s_waitcnt vmcnt(%0)" :: "i"(N) : "memory"); }
DEV void wait_lgkm() { asm volatile("s_waitcnt lgkmcnt(0)" ::: "memory"); }
DEV v4u ldgo(const void* base, unsigned off) { return __builtin_nontemporal_load((const GAS v4u*)((const GAS char*)base + off)); }
DEV v2u trread(LAS unsigned char* p) { return __builtin_bit_cast(v2u, __builtin_amdgcn_ds_read_tr16_b64_v4i16((LAS s16x4*)p)); }
DEV v4u lds128(LAS unsigned char* p) { return *(LAS v4u*)p; }
constexpr int n_issued(int i) { return i < 0 ? 0 : ((i + (i < A_SLOWN ? A_W0 : RS)) < 16 ? (i + (i < A_SLOWN ? A_W0 : RS)) : 16); }
template <int V> struct IC { static constexpr int value = V; };
template <int B, int E, class F> DEV void static_for(F&& f) { if constexpr (B < E) { f(IC<B>{}); static_for<B + 1, E>(f); } }
#define COMPILER_FENCE() asm volatile("" ::: "memory")
#define WAIT_VM(n) __builtin_amdgcn_s_waitcnt(0x0F70 | ((n) & 15) | (((n) >> 4) << 14))
#define DRAIN() asm volatile("s_waitcnt vmcnt(0)" ::: "memory")
DEV void bar() { COMPILER_FENCE(); __builtin_amdgcn_s_waitcnt(0xC07F); __builtin_amdgcn_s_barrier(); COMPILER_FENCE(); }
#define PIN() __builtin_amdgcn_sched_barrier(0)

__global__ void __launch_bounds__(512, 2)
mla_decode_kernel(const u16* __restrict__ x, const u16* __restrict__ ckv, const u16* __restrict__ wq, const u16* __restrict__ wuk,
                  const u16* __restrict__ wuv, const u16* __restrict__ wo, const u16* __restrict__ w1, const u16* __restrict__ b1,
                  const u16* __restrict__ w2, const u16* __restrict__ b2, u16* __restrict__ out, unsigned char* ws) {
    extern __shared__ __attribute__((aligned(16))) unsigned char smem[];
    LAS unsigned char* L = (LAS unsigned char*)smem;
    const int tid = threadIdx.x, lane = tid & 63;
    {
        u64 pc;
        const unsigned line = tid >> 1;
        asm volatile("s_getpc_b64 %0" : "=s"(pc));
        asm volatile("s_mov_b32 m0, %2\n\ts_nop 0\n\tglobal_load_lds_dword %0, %1" :: "v"((line < CODE_LINES ? line : 0u) * 128u), "s"(pc), "i"(LDS_BYTES - 256) : "memory");
    }
    const u64 t_start = __builtin_amdgcn_s_memrealtime();
    asm volatile("" :: "s"(x), "s"(ckv), "s"(wq), "s"(wuk), "s"(wuv), "s"(wo), "s"(w1), "s"(b1), "s"(w2), "s"(b2), "s"(out), "s"(ws));
    u64 epoch = *(const __attribute__((address_space(4))) u64*)(ws + WS_E);
    const int w = __builtin_amdgcn_readfirstlane(tid >> 6);
    const int b = blockIdx.x, xc = b & 7, ii = b >> 3;
    const int n16 = lane & 15, g4 = lane >> 4;
    const rsrc_t rs = __builtin_amdgcn_make_buffer_rsrc(ws, 0, WS_END, 0x00020000);
    const v4u SV = {SENT, SENT, SENT, SENT};
    const float L2E = 1.4426950408889634f;

    if (__builtin_expect(ii < 16, 1)) {
        const int hk = 2 * xc + (ii >> 3), e = ii & 7;
        const int hf = ii, ef = xc;
        const bool has_mlp = ii < 14;
        const int fb = 14 * xc + ii;
        const int wq7 = w < 7 ? w : 6;
        unsigned tag_lo, tag_hi;
        if (w < 4) {
            __builtin_amdgcn_s_setprio(3);
            const v4u xv = ldgo(x, (lane < 56 ? lane : 0) * 16);
            v4u qa[7];
            const unsigned oq = (224 * (ef & 1) + 56 * w + (lane >> 3)) * 8192 + hf * 512 + (ef >> 1) * 128 + (lane & 7) * 16;
#pragma unroll
            for (int u = 0; u < 7; ++u) qa[u] = ldgo(wq, oq + u * 8 * 8192);
            PIN();
            __builtin_amdgcn_s_barrier();
            PIN();
            STAMP(12);
            float xf[8] = {bflo(xv.x), bfhi(xv.x), bflo(xv.y), bfhi(xv.y), bflo(xv.z), bfhi(xv.z), bflo(xv.w), bfhi(xv.w)};
            float s = ((xf[0] + xf[1]) + (xf[2] + xf[3])) + ((xf[4] + xf[5]) + (xf[6] + xf[7]));
            if (lane >= 56) s = 0.f;
            const float mean = wave_sum(s) * (1.f / 448.f);
            float s2 = 0.f;
#pragma unroll
            for (int j = 0; j < 8; ++j) { float d = xf[j] - mean; s2 += d * d; }
            if (lane >= 56) s2 = 0.f;
            const float var = wave_sum(s2) * (1.f / 448.f);
            const float mb = rbf(mean);
            const float den = rbf(__builtin_amdgcn_sqrtf(rbf(rbf(var) + 1e-5f)));
            const float rden = __builtin_amdgcn_rcpf(den);
            if (lane < 56) {
                f32x4 o0, o1;
#pragma unroll
                for (int j = 0; j < 4; ++j) { o0[j] = rbf(rbf(xf[j] - mb) * rden); o1[j] = rbf(rbf(xf[4 + j] - mb) * rden); }
                *(LAS f32x4*)(L + LW_XN + lane * 32) = o0;
                *(LAS f32x4*)(L + LW_XN + lane * 32 + 16) = o1;
            }
            COMPILER_FENCE();
            float xm[7];
#pragma unroll
            for (int u = 0; u < 7; ++u) xm[u] = *(LAS float*)(L + LW_XN + (224 * (ef & 1) + 56 * w + 8 * u + (lane >> 3)) * 4);
            asm volatile("" : "+s"(epoch));
            const u64 tag = epoch * 6364136223846793005ull + 1442695040888963407ull;
            tag_lo = (unsigned)tag; tag_hi = (unsigned)(tag >> 32);
            PIN();
            STAMP(13);
            float acc[8];
#pragma unroll
            for (int j = 0; j < 8; ++j) acc[j] = 0.f;
#pragma unroll
            for (int u = 0; u < 7; ++u) {
                acc[0] += xm[u] * bflo(qa[u].x); acc[1] += xm[u] * bfhi(qa[u].x); acc[2] += xm[u] * bflo(qa[u].y); acc[3] += xm[u] * bfhi(qa[u].y);
                acc[4] += xm[u] * bflo(qa[u].z); acc[5] += xm[u] * bfhi(qa[u].z); acc[6] += xm[u] * bflo(qa[u].w); acc[7] += xm[u] * bfhi(qa[u].w);
                PIN();
            }
            float r0 = add32(acc[0], acc[4]), r1 = add32(acc[1], acc[5]), r2 = add32(acc[2], acc[6]), r3 = add32(acc[3], acc[7]);
            float s0 = add16(r0, r2), s1 = add16(r1, r3);
            s0 += dpp<DPP_ROR8>(s0); s1 += dpp<DPP_ROR8>(s1);
            if ((lane & 8) == 0) {
                f32x2 o = {s0, s1};
                *(LAS f32x2*)(L + LW_QP + w * 256 + (8 * (lane & 7) + 4 * (lane >> 5) + 2 * ((lane >> 4) & 1)) * 4) = o;
            }
            STAMP(1);
            wait_lgkm();
            if (lane == 0) __hip_atomic_fetch_add((LAS unsigned*)(L + LW_CNT), 1u, __ATOMIC_RELAXED, __HIP_MEMORY_SCOPE_WORKGROUP);
            const unsigned th = (tag_lo >> 8) | (tag_hi << 24);
            if (w == 0) {
                for (int spin = 0; spin < (1 << 20) && *(volatile LAS unsigned*)(L + LW_CNT) < 4u; ++spin) {}
                COMPILER_FENCE();
                f32x2 a = {0.f, 0.f};
#pragma unroll
                for (int v = 0; v < 4; ++v) a += *(LAS f32x2*)(L + LW_QP + v * 256 + (lane & 31) * 8);
                const float ax = a.x, ay = a.y;
                v4u gr = {(f2u(ax) >> 8) | (tag_lo << 24), th, (f2u(ay) >> 8) | (tag_lo << 24), th};
                stsc(rs, lane < 32 ? WS_Q1 + hf * 4096 + ef * 512 + lane * 16 : OOB, gr);
                STAMP(2);
            }
            __builtin_amdgcn_s_sleep(H1_SLEEP);
            {
                const int off = WS_Q1 + hf * 4096 + w * 1024 + lane * 8;
                v2u v0, v1;
                v2u fl = ldsc2(rs, WS_FLAG + (w * 32 + (lane & 31)) * 8);
                for (int spin = 0;; ++spin) {
                    v0 = ldsc2(rs, off); v1 = ldsc2(rs, off + 512);
                    const bool ok = ((v0.x ^ (tag_lo << 24)) | (v1.x ^ (tag_lo << 24))) < 0x1000000u && v0.y == th && v1.y == th;
                    if (__all(ok) || spin > SPIN_CAP) break;
                    COMPILER_FENCE();
                }
                for (int spin = 0; !__all(fl.x == tag_lo && fl.y == tag_hi) && spin < SPIN_CAP; ++spin) {
                    __builtin_amdgcn_s_sleep(8);
                    fl = ldsc2(rs, WS_FLAG + (w * 32 + (lane & 31)) * 8);
                    COMPILER_FENCE();
                }
                const float qv = u2f(v0.x << 8) + u2f(v1.x << 8);
                *(LAS u16*)(L + LW_QV + (w * 64 + lane) * 2) = (u16)pk2(qv, qv);
            }
            STAMP(3);
            __builtin_amdgcn_s_setprio(0);
            bar();
        } else {
            const int kw = w - 4;
            if (kw == 0) { const v2u z = {0u, 0u}; *(LAS v2u*)(L + LW_Z + lane * 8) = z; *(LAS unsigned*)(L + LW_CNT) = 0u; }
            PIN();
            bar();
            v4u wk[16];
            const unsigned ok = (128 * ef + 32 * kw + (n16 >> 2)) * 8192 + hf * 512 + g4 * 64 + (n16 & 3) * 16;
#pragma unroll
            for (int u = 0; u < 16; ++u) wk[u] = ldgo(wuk, ok + (u >> 1) * 4 * 8192 + (u & 1) * 256);
            PIN();
            asm volatile("" : "+s"(epoch));
            const u64 tag = epoch * 6364136223846793005ull + 1442695040888963407ull;
            tag_lo = (unsigned)tag; tag_hi = (unsigned)(tag >> 32);
            STAMP(1);
            bar();
            v4u qf[8];
#pragma unroll
            for (int u = 0; u < 8; ++u) qf[u] = lds128(L + ((n16 >> 2) == (u >> 1) ? LW_QV : LW_Z) + (u & 1) * 256 + g4 * 64 + (n16 & 3) * 16);
            PIN();
            f32x4 d0 = {0.f, 0.f, 0.f, 0.f}, d1 = {0.f, 0.f, 0.f, 0.f};
#pragma unroll
            for (int u = 0; u < 8; ++u) d0 = mfma(wk[u], qf[u], d0);
#pragma unroll
            for (int u = 0; u < 8; ++u) d1 = mfma(wk[8 + u], qf[u], d1);
            float v0 = (lane & 2) ? ((lane & 1) ? d0.w : d0.z) : ((lane & 1) ? d0.y : d0.x);
            float v1 = (lane & 2) ? ((lane & 1) ? d1.w : d1.z) : ((lane & 1) ? d1.y : d1.x);
            v0 += dpp<DPP_X1>(v0); v1 += dpp<DPP_X1>(v1); v0 += dpp<DPP_X2>(v0); v1 += dpp<DPP_X2>(v1);
            const float v = ((lane & 1) ? v1 : v0) * 0.0625f;
            unsigned vb = pk2(v, v) & 0xffffu;
            vb = vb < 0xfffeu ? vb : 0xfffeu;
            if ((lane & 2) == 0) *(LAS u16*)(L + LW_QA + (32 * kw + 16 * (lane & 1) + (n16 & 12) + g4) * 2) = (u16)vb;
            STAMP(3);
        }
        const u64 tag = ((u64)tag_hi << 32) | tag_lo;
        bar();
        if (w == 4) stsc(rs, lane < 16 ? WS_Q2 + hf * 2048 + ef * 256 + lane * 16 : OOB, lds128(L + LW_QA + n16 * 16));
        PIN();
        STAMP(4);
        {
            const unsigned vo = (512 * hk + 64 * w + g4) * 2048 + e * 256;
#pragma unroll
            for (int u = 0; u < 16; ++u) {
                if (u >= COLS_WIN) wait_vm<COLS_WIN - 1>();
                dmas<0>(ckv, vo + u * 8192 + ((n16 ^ ((g4 << 2) | (u & 3))) * 16), LW_COLS + (64 * w + 4 * u) * 256);
            }
        }
        PIN();
        STAMP(5);
        wait_vm<0>();
        bar();
        STAMP(6);
        v4u vf[16];
        {
            const int q = n16 >> 2, p = lane & 3;
#pragma unroll
            for (int t = 0; t < 2; ++t) {
                LAS unsigned char* base = L + LW_COLS + (8 * g4 + 4 * t + q) * 256 + (((2 * w + (p >> 1)) ^ ((q << 2) | ((2 * g4 + t) & 3))) * 16) + 8 * (p & 1);
#pragma unroll
                for (int ks = 0; ks < 16; ++ks) {
                    v2u r = trread(base + ks * 8192);
                    if (t == 0) { vf[ks].x = r.x; vf[ks].y = r.y; } else { vf[ks].z = r.x; vf[ks].w = r.y; }
                }
            }
        }
        PIN();
        const int off3 = WS_S3 + (hk * 32 + (lane >> 2)) * 512 + w * 64 + (lane & 3) * 16;
        if (w == 0) {
            for (int spin = 0; spin < SPIN_CAP; ++spin) {
                const unsigned hv = __builtin_amdgcn_raw_buffer_load_b32(rs, WS_H3 + (hk * 8 + (lane & 7)) * 128, 0, 16);
                if (__all(hv != SENT)) break;
                COMPILER_FENCE();
            }
            if (H3_SLEEP) __builtin_amdgcn_s_sleep(H3_SLEEP);
        }
        STAMP(14);
        bar();
        v4u sc0 = ldsc(rs, off3), sc1 = ldsc(rs, off3 + 16 * 512);
        for (int spin = 0; spin < SPIN_CAP; ++spin) {
            const bool ok0 = __all(umax4(sc0) != SENT), ok1 = __all(umax4(sc1) != SENT);
            if (ok0 && ok1) break;
            if (!ok0) sc0 = ldsc(rs, off3);
            if (!ok1) sc1 = ldsc(rs, off3 + 16 * 512);
            COMPILER_FENCE();
        }
        STAMP(7);
        float sm_m, sm_l;
        {
            float s[16] = {bflo(sc0.x), bfhi(sc0.x), bflo(sc0.y), bfhi(sc0.y), bflo(sc0.z), bfhi(sc0.z), bflo(sc0.w), bfhi(sc0.w),
                           bflo(sc1.x), bfhi(sc1.x), bflo(sc1.y), bfhi(sc1.y), bflo(sc1.z), bfhi(sc1.z), bflo(sc1.w), bfhi(sc1.w)};
            float m = s[0];
#pragma unroll
            for (int j = 1; j < 16; ++j) m = fmaxf(m, s[j]);
            m = fmaxf(m, dpp<DPP_X1>(m)); m = fmaxf(m, dpp<DPP_ROR4>(m)); m = fmaxf(m, dpp<DPP_ROR8>(m));
            m = max16(m); m = max32(m);
            const float mm = -m * L2E;
#pragma unroll
            for (int j = 0; j < 16; ++j) s[j] = __builtin_amdgcn_exp2f(__builtin_fmaf(s[j], L2E, mm));
            v4u p0 = {pk2(s[0], s[1]), pk2(s[2], s[3]), pk2(s[4], s[5]), pk2(s[6], s[7])};
            v4u p1 = {pk2(s[8], s[9]), pk2(s[10], s[11]), pk2(s[12], s[13]), pk2(s[14], s[15])};
            const int tile = lane >> 2, head = 2 * w + ((lane >> 1) & 1), gp = 2 * (tile & 1) + (lane & 1);
            LAS unsigned char* dst = L + LW_PF + (tile >> 1) * 1024 + head * 64 + ((gp ^ ((-(head >> 2)) & 3)) * 16);
            *(LAS v4u*)dst = p0;
            *(LAS v4u*)(dst + 8192) = p1;
            PIN();
            sm_m = m;
            sm_l = (((s[0] + s[1]) + (s[2] + s[3])) + ((s[4] + s[5]) + (s[6] + s[7]))) + (((s[8] + s[9]) + (s[10] + s[11])) + ((s[12] + s[13]) + (s[14] + s[15])));
        }
        bar();
        v4u m1[2], m2[2];
        unsigned b1v = 0;
        {
            v4u pf[16];
            LAS unsigned char* pb = L + LW_PF + n16 * 64 + ((g4 ^ ((-(n16 >> 2)) & 3)) * 16);
#pragma unroll
            for (int ks = 0; ks < 16; ++ks) pf[ks] = lds128(pb + ks * 1024);
            PIN();
            f32x4 d = {0.f, 0.f, 0.f, 0.f};
#pragma unroll
            for (int ks = 0; ks < 16; ++ks) d = mfma(vf[ks], pf[ks], d);
            float l = sm_l;
            l += dpp<DPP_X1>(l); l += dpp<DPP_ROR4>(l); l += dpp<DPP_ROR8>(l); l = add16(l, l); l = add32(l, l);
            if ((lane & 61) == 0) { v2u o = {nosent(f2u(sm_m)), nosent(f2u(l))}; *(LAS v2u*)(L + LW_ST + (2 * w + (lane >> 1)) * 8) = o; }
            v2u o = {nosent(pk2(d.x, d.y)), nosent(pk2(d.z, d.w))};
            *(LAS v2u*)(L + LW_TR + n16 * 272 + w * 32 + g4 * 8) = o;
        }
        bar();
        if (w < 4) stsc(rs, WS_P4 + ((4 * w + g4) * 16 + hk) * 2048 + e * 256 + n16 * 16, lds128(L + LW_TR + (4 * w + g4) * 272 + n16 * 16));
        else if (w == 4 && e == 0) { if (lane < 8) stsc(rs, WS_ST4 + hk * 128 + lane * 16, lds128(L + LW_ST + lane * 16)); }
        PIN();
        STAMP(8);
        if (has_mlp) {
            {
                const char* p1 = (const char*)w1 + (size_t)(64 * wq7 + (lane >> 1)) * 3584 + fb * 32 + (lane & 1) * 16;
                m1[0] = ldg(p1); m1[1] = ldg(p1 + 32 * 3584);
                const char* p2 = (const char*)w2 + (size_t)(fb * 16 + 2 * w) * 896 + (lane < 56 ? lane : 0) * 16;
                m2[0] = ldg(p2); m2[1] = ldg(p2 + 896);
                b1v = *(const GAS unsigned*)((const char*)b1 + fb * 32 + w * 4);
            }
            PIN();
            if (w < 7) {
                float acc[8];
#pragma unroll
                for (int j = 0; j < 8; ++j) acc[j] = 0.f;
#pragma unroll
                for (int u = 0; u < 2; ++u) {
                    const float xm = *(LAS float*)(L + LW_XN + (64 * w + 32 * u + (lane >> 1)) * 4);
                    acc[0] += xm * bflo(m1[u].x); acc[1] += xm * bfhi(m1[u].x); acc[2] += xm * bflo(m1[u].y); acc[3] += xm * bfhi(m1[u].y);
                    acc[4] += xm * bflo(m1[u].z); acc[5] += xm * bfhi(m1[u].z); acc[6] += xm * bflo(m1[u].w); acc[7] += xm * bfhi(m1[u].w);
                }
                float r0 = add32(acc[0], acc[4]), r1 = add32(acc[1], acc[5]), r2 = add32(acc[2], acc[6]), r3 = add32(acc[3], acc[7]);
                float s0 = add16(r0, r2), s1 = add16(r1, r3);
                s0 += dpp<DPP_X2>(s0); s1 += dpp<DPP_X2>(s1);
                s0 += dpp<DPP_ROR4>(s0); s1 += dpp<DPP_ROR4>(s1); s0 += dpp<DPP_ROR8>(s0); s1 += dpp<DPP_ROR8>(s1);
                if ((lane & 14) == 0) {
                    f32x2 o = {s0, s1};
                    *(LAS f32x2*)(L + LW_HP + w * 64 + (8 * (lane & 1) + 4 * (lane >> 5) + 2 * ((lane >> 4) & 1)) * 4) = o;
                }
            }
            bar();
            f32x2 hh = {0.f, 0.f};
#pragma unroll
            for (int v = 0; v < 7; ++v) hh += *(LAS f32x2*)(L + LW_HP + v * 64 + w * 8);
            float h0 = rbf(rbf(hh.x) + bflo(b1v)), h1 = rbf(rbf(hh.y) + bfhi(b1v));
            h0 = h0 > 0.f ? h0 : 0.f; h1 = h1 > 0.f ? h1 : 0.f;
            h0 = rbf(h0 * h0); h1 = rbf(h1 * h1);
            if (lane < 56) {
                f32x4 o0 = {h0 * bflo(m2[0].x) + h1 * bflo(m2[1].x), h0 * bfhi(m2[0].x) + h1 * bfhi(m2[1].x),
                            h0 * bflo(m2[0].y) + h1 * bflo(m2[1].y), h0 * bfhi(m2[0].y) + h1 * bfhi(m2[1].y)};
                f32x4 o1 = {h0 * bflo(m2[0].z) + h1 * bflo(m2[1].z), h0 * bfhi(m2[0].z) + h1 * bfhi(m2[1].z),
                            h0 * bflo(m2[0].w) + h1 * bflo(m2[1].w), h0 * bfhi(m2[0].w) + h1 * bfhi(m2[1].w)};
                *(LAS f32x4*)(L + LW_COLS + w * 1792 + lane * 32) = o0;
                *(LAS f32x4*)(L + LW_COLS + w * 1792 + lane * 32 + 16) = o1;
            }
            bar();
            if (tid < 112) {
                f32x4 a = {0.f, 0.f, 0.f, 0.f};
#pragma unroll
                for (int v = 0; v < 8; ++v) a += *(LAS f32x4*)(L + LW_COLS + v * 1792 + tid * 16);
                float a0 = a.x, a1 = a.y, a2 = a.z, a3 = a.w;
                v4u o = {nosent(f2u(a0)), nosent(f2u(a1)), nosent(f2u(a2)), nosent(f2u(a3))};
                stsc(rs, WS_M5 + (tid >> 3) * (112 * 128) + fb * 128 + (tid & 7) * 16, o);
            }
            STAMP(9);
        } else {
            const int fi = 2 * xc + (ii - 14);
            if (fi < 14) {
                const unsigned xb = *(const GAS u16*)(x + fi * 32 + (lane & 31)), b2b = *(const GAS u16*)(b2 + fi * 32 + (lane & 31));
                const int offm = WS_M5 + fi * (112 * 128) + wq7 * 2048 + lane * 16, offs = WS_P5 + fi * 16384 + w * 2048 + lane * 16;
                v4u a0 = ldsc(rs, offm), a1 = ldsc(rs, offm + 1024), c0 = ldsc(rs, offs), c1 = ldsc(rs, offs + 1024);
                for (int spin = 0; spin < SPIN_CAP; ++spin) {
                    const bool k0 = __all(umax4(a0) != SENT), k1 = __all(umax4(a1) != SENT), k2 = __all(umax4(c0) != SENT), k3 = __all(umax4(c1) != SENT);
                    if (k0 && k1 && k2 && k3) break;
                    if (!k0) a0 = ldsc(rs, offm);
                    if (!k1) a1 = ldsc(rs, offm + 1024);
                    if (!k2) c0 = ldsc(rs, offs);
                    if (!k3) c1 = ldsc(rs, offs + 1024);
                    COMPILER_FENCE();
                }
                STAMP(10);
                {
                    const f32x4 am = __builtin_bit_cast(f32x4, a0) + __builtin_bit_cast(f32x4, a1);
                    const f32x4 as = __builtin_bit_cast(f32x4, c0) + __builtin_bit_cast(f32x4, c1);
                    float s0 = add16(add32(am.x, am.z), add32(am.y, am.w)), s1 = add16(add32(as.x, as.z), add32(as.y, as.w));
                    s0 += dpp<DPP_ROR8>(s0); s1 += dpp<DPP_ROR8>(s1);
                    f32x2 o = {s0, s1};
                    if ((lane & 8) == 0) *(LAS f32x2*)(L + LW_FR + w * 256 + ((lane & 7) * 4 + 2 * (lane >> 5) + ((lane >> 4) & 1)) * 8) = o;
                }
                bar();
                if (tid < 32) {
                    f32x2 a = {0.f, 0.f};
#pragma unroll
                    for (int v = 0; v < 7; ++v) a += *(LAS f32x2*)(L + LW_FR + v * 256 + tid * 8);
                    const float sv = a.y + *(LAS float*)(L + LW_FR + 7 * 256 + tid * 8 + 4);
                    const float o2 = rbf(rbf(a.x) + bflo(b2b));
                    const float gate = rbf(o2 * __builtin_amdgcn_rcpf(1.0f + __builtin_amdgcn_exp2f(-o2 * L2E)));
                    const float o = bflo(xb) + rbf(rbf(sv) * gate);
                    *(GAS u16*)(out + fi * 32 + tid) = (u16)pk2(o, o);
                    if (fi == 0 && tid == 0) *(GAS u64*)(ws + WS_E) = tag;
                }
                STAMP(11);
            }
        }
    } else {
        const int ia = ii - 16, ai = xc * 16 + ia;
        const int hd = 2 * xc + (ia >> 3), ch = (ia >> 2) & 1, dq = ia & 3;
        if (w < 4) {
            v4u a[32], q[16];
            const int r8 = lane >> 3, pos = lane & 7;
            const unsigned vo0 = (64 * ai + 16 * w + r8) * 2048 + ((pos ^ (r8 >> 1)) * 16);
            const unsigned vo1 = (64 * ai + 16 * w + 8 + r8) * 2048 + ((pos ^ (4 + (r8 >> 1))) * 16);
            const int ring = LA_RING + w * RS * 2048;
            LAS unsigned char* ra0 = L + ring + n16 * 128 + ((g4 ^ (n16 >> 1)) * 16);
            LAS unsigned char* ra1 = L + ring + n16 * 128 + (((4 + g4) ^ (n16 >> 1)) * 16);
            LAS unsigned char* qb = L + LA_Q + n16 * 64 + ((g4 ^ ((-(n16 >> 2)) & 3)) * 16);
            f32x4 d = {0.f, 0.f, 0.f, 0.f};
            static_for<0, 16>([&](auto I) {
                constexpr int i = I.value;
                static_for<n_issued(i - 1), n_issued(i)>([&](auto J) {
                    constexpr int j = J.value;
                    dmas<j * 128>(ckv, vo0, ring + (j % RS) * 2048 - j * 128);
                    dmas<j * 128>(ckv, vo1, ring + (j % RS) * 2048 + 1024 - j * 128);
                });
                if constexpr (n_issued(i) == 16 && n_issued(i - 1) < 16) {
                    STAMP(1);
                    bar();
                    STAMP(2);
#pragma unroll
                    for (int j = 0; j < 16; ++j) q[j] = lds128(qb + j * 1024);
                    PIN();
#pragma unroll
                    for (int j = 0; j < 16; ++j) d = mfma(a[j], q[j], d);
                    PIN();
#pragma unroll
                    for (int j = 0; j < 16; ++j) q[j] = lds128(qb + (16 + j) * 1024);
                    PIN();
#pragma unroll
                    for (int j = 16; j < 2 * i; ++j) d = mfma(a[j], q[j - 16], d);
                    PIN();
                }
                wait_vm<2 * (n_issued(i) - i - 1)>();
                a[2 * i] = lds128(ra0 + (i % RS) * 2048);
                a[2 * i + 1] = lds128(ra1 + (i % RS) * 2048);
                if constexpr (n_issued(i) < 16) {
                    asm volatile("s_waitcnt lgkmcnt(0)" : "+v"(a[2 * i]), "+v"(a[2 * i + 1]) :: "memory");
                } else {
                    d = mfma(a[2 * i], q[2 * i - 16], d);
                    d = mfma(a[2 * i + 1], q[2 * i + 1 - 16], d);
                    PIN();
                }
            });
            unsigned a0 = nosent(pk2(d.x, d.y)), a1 = nosent(pk2(d.z, d.w)), c0 = a0, c1 = a1;
            swap16(a0, c0); swap16(a1, c1);
            v4u o = {a0, a1, c0, c1};
            stsc(rs, (g4 & 1) == 0 ? WS_S3 + (ai * 4 + w) * 512 + n16 * 32 + (g4 >> 1) * 16 : OOB, o);
            PIN();
            STAMP(3);
        } else {
            const int tw = w - 4;
            if (tw == 0) {
#pragma unroll
                for (int u = 0; u < 10; ++u) stsc(rs, WS_Q2 + ai * 10240 + u * 1024 + lane * 16, SV);
            }
            PIN();
            asm volatile("" : "+s"(epoch));
            const u64 tag = epoch * 6364136223846793005ull + 1442695040888963407ull;
            const unsigned tag_lo = (unsigned)tag, tag_hi = (unsigned)(tag >> 32);
            if (tw == 0) {
                wait_vm<0>();
                v2u t = {tag_lo, tag_hi};
                stsc2(rs, lane == 0 ? WS_FLAG + ai * 8 : OOB, t);
            }
            STAMP(1);
            while (__builtin_amdgcn_s_memrealtime() - t_start < FLAG_TICKS) __builtin_amdgcn_s_sleep(2);
            for (int spin = 0;; ++spin) {
                v4u v = ldsc(rs, WS_FLAG + lane * 16);
                const bool ok = v.x == tag_lo && v.y == tag_hi && v.z == tag_lo && v.w == tag_hi;
                if (__all(ok) || spin > SPIN_CAP) break;
                __builtin_amdgcn_s_sleep(8);
                COMPILER_FENCE();
            }
            STAMP(2);
            while (__builtin_amdgcn_s_memrealtime() - t_start < H2_TICKS) __builtin_amdgcn_s_sleep(1);
            for (int spin = 0; spin < SPIN_CAP; ++spin) {
                v4u v = ldsc(rs, WS_Q2 + tw * 8192 + (ia & 7) * 1024 + lane * 16);
                if (__all(umax4(v) != SENT)) break;
                COMPILER_FENCE();
            }
            STAMP(12);
            {
                v4u qv[8];
                const int off = WS_Q2 + tw * 8192 + lane * 16;
#pragma unroll
                for (int u = 0; u < 8; ++u) qv[u] = ldsc(rs, off + u * 1024);
                for (int spin = 0; spin < SPIN_CAP; ++spin) {
                    bool all = true;
#pragma unroll
                    for (int u = 0; u < 8; ++u) {
                        if (!__all(umax4(qv[u]) != SENT)) { all = false; qv[u] = ldsc(rs, off + u * 1024); }
                    }
                    if (all) break;
                    COMPILER_FENCE();
                }
                LAS unsigned char* dst = L + LA_Q + (lane >> 2) * 1024 + tw * 256 + (((lane & 3) ^ ((-tw) & 3)) * 16);
#pragma unroll
                for (int u = 0; u < 8; ++u) *(LAS v4u*)(dst + (u & 1) * 16384 + (u >> 1) * 64) = qv[u];
            }
            STAMP(3);
            bar();
            if (tw == 0) __builtin_amdgcn_raw_buffer_store_b32(0u, rs, lane == 0 ? WS_H3 + ai * 128 : OOB, 0, 16);
            {
                const int r8 = lane >> 3, pos = lane & 7;
                const unsigned vo = (512 * ch + 128 * tw + r8) * 8192 + hd * 512 + dq * 128;
#pragma unroll
                for (int u = 0; u < TAIL_X; ++u)
                    dmas<0>(wuv, vo + u * 65536 + ((pos ^ ((((r8 >> 1) & 1) | ((u & 1) << 1)) << 1)) * 16), LA_UV + (128 * tw + 8 * u) * 128);
            }
            STAMP(4);
        }
        bar();
        {
            const int r8 = lane >> 3, pos = lane & 7, tq = w & 3, th = w >> 2;
            const unsigned vo = (512 * ch + 128 * tq + r8) * 8192 + hd * 512 + dq * 128;
#pragma unroll
            for (int v = 0; v < (16 - TAIL_X) / 2; ++v) {
                const int u = TAIL_X + 2 * v + th;
                dmas<0>(wuv, vo + u * 65536 + ((pos ^ ((((r8 >> 1) & 1) | ((u & 1) << 1)) << 1)) * 16), LA_UV + (128 * tq + 8 * u) * 128);
            }
            const unsigned so = (hd * 256 + dq * 64 + 8 * w) * 896 + lane * 16;
            if (lane < 56) {
#pragma unroll
                for (int u = 0; u < 8; ++u) dmas<0>(wo, so + u * 896, LA_WO + (8 * w + u) * 928 + w * 128);
            }
        }
        PIN();
        wait_vm<0>();
        STAMP(5);
        bar();
        const int off4 = WS_P4 + (hd * 16 + (lane >> 3)) * 2048 + ch * 1024 + w * 128 + (lane & 7) * 16;
        const int soff = WS_ST4 + g4 * 512 + hd * 8;
        v2u s0 = ldsc2(rs, soff), s1 = ldsc2(rs, soff + 128), s2 = ldsc2(rs, soff + 256), s3 = ldsc2(rs, soff + 384);
        v4u c0 = ldsc(rs, off4), c1 = ldsc(rs, off4 + 8 * 2048);
        PIN();
        v4u uf[8], of[8];
        {
            const int q = n16 >> 2, p = lane & 3;
#pragma unroll
            for (int t = 0; t < 2; ++t) {
                const int sw = ((q >> 1) | ((g4 & 1) << 1)) << 1;
                LAS unsigned char* ub = L + LA_UV + (64 * w + 8 * g4 + 4 * t + q) * 128 + 8 * (p & 1);
                const int wq7 = w < 7 ? w : 6;
                LAS unsigned char* ob = L + LA_WO + (8 * g4 + 4 * t + q) * 928 + g4 * 128 + wq7 * 128 + p * 8;
#pragma unroll
                for (int ks = 0; ks < 2; ++ks)
#pragma unroll
                    for (int ct = 0; ct < 4; ++ct) {
                        v2u r = trread(ub + ks * 4096 + (((2 * ct + (p >> 1)) ^ sw) * 16));
                        v2u o = trread(ob + ks * (32 * 928 + 512) + ct * 32);
                        if (t == 0) { uf[ks * 4 + ct].x = r.x; uf[ks * 4 + ct].y = r.y; of[ks * 4 + ct].x = o.x; of[ks * 4 + ct].y = o.y; }
                        else { uf[ks * 4 + ct].z = r.x; uf[ks * 4 + ct].w = r.y; of[ks * 4 + ct].z = o.x; of[ks * 4 + ct].w = o.y; }
                    }
            }
        }
        PIN();
        STAMP(6);
        {
            for (int spin = 0; spin < SPIN_CAP; ++spin) {
                const bool ks = __all(max(max(max(s0.x, s0.y), max(s1.x, s1.y)), max(max(s2.x, s2.y), max(s3.x, s3.y))) != SENT);
                const bool kc = __all(max(umax4(c0), umax4(c1)) != SENT);
                if (ks && kc) break;
                if (!ks) { s0 = ldsc2(rs, soff); s1 = ldsc2(rs, soff + 128); s2 = ldsc2(rs, soff + 256); s3 = ldsc2(rs, soff + 384); }
                if (!kc) { c0 = ldsc(rs, off4); c1 = ldsc(rs, off4 + 8 * 2048); }
                COMPILER_FENCE();
            }
            STAMP(7);
            {
                LAS unsigned char* bn = L + LA_BN + w * 2304;
                *(LAS v4u*)(bn + (lane >> 3) * 144 + (lane & 7) * 16) = c0;
                *(LAS v4u*)(bn + (8 + (lane >> 3)) * 144 + (lane & 7) * 16) = c1;
                COMPILER_FENCE();
                c0 = lds128(bn + n16 * 144 + g4 * 16);
                c1 = lds128(bn + n16 * 144 + 64 + g4 * 16);
            }
            f32x4 d[4];
#pragma unroll
            for (int ct = 0; ct < 4; ++ct) {
                f32x4 z = {0.f, 0.f, 0.f, 0.f};
                d[ct] = mfma(c0, uf[ct], z);
            }
#pragma unroll
            for (int ct = 0; ct < 4; ++ct) d[ct] = mfma(c1, uf[4 + ct], d[ct]);
            const float m0 = u2f(s0.x), m1 = u2f(s1.x), m2 = u2f(s2.x), m3 = u2f(s3.x);
            float M = fmaxf(fmaxf(m0, m1), fmaxf(m2, m3));
            M = max16(M); M = max32(M);
            float f0 = __builtin_amdgcn_exp2f((m0 - M) * L2E), f1 = __builtin_amdgcn_exp2f((m1 - M) * L2E);
            float f2 = __builtin_amdgcn_exp2f((m2 - M) * L2E), f3 = __builtin_amdgcn_exp2f((m3 - M) * L2E);
            float Ls = (f0 * u2f(s0.y) + f1 * u2f(s1.y)) + (f2 * u2f(s2.y) + f3 * u2f(s3.y));
            Ls = add16(Ls, Ls); Ls = add32(Ls, Ls);
            const float rl = __builtin_amdgcn_rcpf(Ls);
            f0 *= rl; f1 *= rl; f2 *= rl; f3 *= rl;
            float cx[4];
#pragma unroll
            for (int ct = 0; ct < 4; ++ct) cx[ct] = (f0 * d[ct].x + f1 * d[ct].y) + (f2 * d[ct].z + f3 * d[ct].w);
            const float r0 = add32(cx[0], cx[2]), r1 = add32(cx[1], cx[3]);
            const float cv = add16(r0, r1);
            const float hi = rbf(cv), lo = cv - hi;
            const unsigned pk = pk2(hi, lo);
            *(LAS u16*)(L + LA_CT + w * 144 + lane * 2) = (u16)pk;
            *(LAS u16*)(L + LA_CT + (8 + w) * 144 + lane * 2) = (u16)(pk >> 16);
        }
        bar();
        if (w < 7) {
            const v4u a0 = lds128(L + LA_CT + n16 * 144 + g4 * 16), a1 = lds128(L + LA_CT + n16 * 144 + 64 + g4 * 16);
            f32x4 d[4];
#pragma unroll
            for (int ct = 0; ct < 4; ++ct) {
                f32x4 z = {0.f, 0.f, 0.f, 0.f};
                d[ct] = mfma(a0, of[ct], z);
            }
#pragma unroll
            for (int ct = 0; ct < 4; ++ct) d[ct] = mfma(a1, of[4 + ct], d[ct]);
            float sx[4];
#pragma unroll
            for (int ct = 0; ct < 4; ++ct) sx[ct] = (d[ct].x + d[ct].y) + (d[ct].z + d[ct].w);
            const float r0 = add32(sx[0], sx[2]), r1 = add32(sx[1], sx[3]);
            const float sv = add16(r0, r1);
            const unsigned su = nosent(f2u(sv));
            v4u o;
            o.x = su;
            o.y = (unsigned)__builtin_amdgcn_update_dpp(0, (int)su, 0x55, 0xf, 0xf, true);
            o.z = (unsigned)__builtin_amdgcn_update_dpp(0, (int)su, 0xAA, 0xf, 0xf, true);
            o.w = (unsigned)__builtin_amdgcn_update_dpp(0, (int)su, 0xFF, 0xf, 0xf, true);
            stsc(rs, (lane & 3) == 0 ? WS_P5 + (2 * w + (lane >> 5)) * 16384 + ai * 128 + (lane & 31) * 4 : OOB, o);
        }
        STAMP(8);
    }
    STAMP_OUT();
}

extern "C" void kernel_launch(void* const* d_in, const int* in_sizes, int n_in, void* d_out, int out_size, void* d_ws, size_t ws_size,
                              hipStream_t stream) {
    (void)hipFuncSetAttribute((const void*)mla_decode_kernel, hipFuncAttributeMaxDynamicSharedMemorySize, LDS_BYTES);
    mla_decode_kernel<<<256, 512, LDS_BYTES, stream>>>((const u16*)d_in[0], (const u16*)d_in[1], (const u16*)d_in[2], (const u16*)d_in[3],
                                                       (const u16*)d_in[4], (const u16*)d_in[5], (const u16*)d_in[7], (const u16*)d_in[8],
                                                       (const u16*)d_in[9], (const u16*)d_in[10], (u16*)d_out, (unsigned char*)d_ws);
}
```

```cpp
#define AMP 99
#define AMPB 112
#define AMPW 0
#include <hip/hip_runtime.h>
#include <hip/hip_bf16.h>

typedef unsigned short u16;
typedef unsigned long long u64;
typedef unsigned v4u __attribute__((ext_vector_type(4)));
typedef unsigned v2u __attribute__((ext_vector_type(2)));
typedef float f32x4 __attribute__((ext_vector_type(4)));
typedef float f32x2 __attribute__((ext_vector_type(2)));
typedef __bf16 bf16x8 __attribute__((ext_vector_type(8)));
typedef __bf16 bf16x2 __attribute__((ext_vector_type(2)));
typedef short s16x4 __attribute__((ext_vector_type(4)));
#define LAS __attribute__((address_space(3)))
#define GAS __attribute__((address_space(1)))
#define DEV __device__ __forceinline__
typedef __amdgpu_buffer_rsrc_t rsrc_t;

constexpr int WS_E = 0;
constexpr int WS_FLAG = 4096;
constexpr int WS_Q1 = 8192;
constexpr int WS_Q2 = WS_Q1 + 65536;
constexpr int WS_S3 = WS_Q2 + 32768;
constexpr int WS_P4 = WS_S3 + 262144;
constexpr int WS_ST4 = WS_P4 + 524288;
constexpr int WS_P5 = WS_ST4 + 4096;
constexpr int WS_M5 = WS_P5 + 14 * 16384;
constexpr int WS_H3 = WS_M5 + 14 * 112 * 128;
constexpr int WS_END = WS_Q2 + 128 * 10240;
static_assert(WS_H3 + 128 * 128 <= WS_END, "workspace map");
constexpr unsigned SENT = 0xffffffffu;
constexpr int SPIN_CAP = 1 << 14;
constexpr int OOB = 0x7ffffff0;
constexpr unsigned CODE_LINES = 145;

constexpr int LW_COLS = 0;
constexpr int LW_PF = 131072;
constexpr int LW_XN = LW_PF + 16384;
constexpr int LW_QP = LW_XN + 2048;
constexpr int LW_QV = LW_QP + 1024;
constexpr int LW_HP = LW_QV + 512;
constexpr int LW_FR = LW_HP + 512;
constexpr int LW_TR = LW_FR + 2048;
constexpr int LW_ST = LW_TR + 16 * 272;
constexpr int LW_QA = LW_ST + 128;
constexpr int LW_Z = LW_QA + 256;
constexpr int LW_CNT = LW_Z + 512;
#define RS 4
constexpr int LA_Q = 0;
constexpr int LA_RING = 32768;
constexpr int LA_WO = 0;
constexpr int LA_BN = LA_WO + 60416;
constexpr int LA_CT = LA_BN + 8 * 2304;
constexpr int LA_UV = 83968; static_assert(LA_RING + 4 * RS * 2048 <= 83968, "ring");
static_assert(LA_CT + 2304 <= LA_UV && LA_UV + 65536 <= 161280 - 256, "A LDS map");
constexpr int LDS_BYTES = 161280;

#define A_W0 1
#define A_SLOWN 2
#define EARLY_COLS 0
#define H1_SLEEP 8
#define H3_SLEEP 0
#define TAIL_X 4
#define COLS_WIN 16
#define H2_TICKS 280
#define FLAG_TICKS 150
#define STAMP(i) do { if (i == AMP) { __builtin_amdgcn_sched_barrier(0); asm volatile("s_memrealtime %0\n\ts_waitcnt lgkmcnt(0)" : "=s"(amp_t) :: "memory"); __builtin_amdgcn_sched_barrier(0); } } while (0)
#define STAMP_OUT() do { if (b == AMPB && w == AMPW) { const u64 n0 = __builtin_amdgcn_s_memrealtime(); const u64 d = 10 * (amp_t - t_start); while (__builtin_amdgcn_s_memrealtime() - n0 < d) __builtin_amdgcn_s_sleep(1); } } while (0)

DEV unsigned f2u(float f) { return __builtin_bit_cast(unsigned, f); }
DEV float u2f(unsigned u) { return __builtin_bit_cast(float, u); }
DEV float bflo(unsigned w) { return u2f(w << 16); }
DEV float bfhi(unsigned w) { return u2f(w & 0xffff0000u); }
DEV unsigned pk2(float a, float b) { f32x2 p = {a, b}; bf16x2 r = __builtin_convertvector(p, bf16x2); return __builtin_bit_cast(unsigned, r); }
DEV float rbf(float a) { return bflo(pk2(a, a)); }
DEV unsigned nosent(unsigned u) { return u < 0xfffffffeu ? u : 0xfffffffeu; }
DEV f32x4 mfma(v4u a, v4u b, f32x4 c) {
    return __builtin_amdgcn_mfma_f32_16x16x32_bf16(__builtin_bit_cast(bf16x8, a), __builtin_bit_cast(bf16x8, b), c, 0, 0, 0);
}
template <int CTRL> DEV float dpp(float v) { return u2f((unsigned)__builtin_amdgcn_update_dpp(0, (int)f2u(v), CTRL, 0xf, 0xf, true)); }
constexpr int DPP_X1 = 0xB1, DPP_X2 = 0x4E, DPP_ROR4 = 0x124, DPP_ROR8 = 0x128;
DEV void swap16(unsigned& a, unsigned& b) { auto r = __builtin_amdgcn_permlane16_swap(a, b, false, false); a = r[0]; b = r[1]; }
DEV void swap32(unsigned& a, unsigned& b) { auto r = __builtin_amdgcn_permlane32_swap(a, b, false, false); a = r[0]; b = r[1]; }
DEV float add16(float a, float b) { unsigned x = f2u(a), y = f2u(b); swap16(x, y); return u2f(x) + u2f(y); }
DEV float add32(float a, float b) { unsigned x = f2u(a), y = f2u(b); swap32(x, y); return u2f(x) + u2f(y); }
DEV float max16(float a) { unsigned x = f2u(a), y = x; swap16(x, y); return fmaxf(u2f(x), u2f(y)); }
DEV float max32(float a) { unsigned x = f2u(a), y = x; swap32(x, y); return fmaxf(u2f(x), u2f(y)); }
DEV float wave_sum(float v) {
    v += dpp<DPP_X1>(v); v += dpp<DPP_X2>(v); v += dpp<DPP_ROR4>(v); v += dpp<DPP_ROR8>(v);
    v = add16(v, v); v = add32(v, v); return v;
}
DEV v4u ldsc(rsrc_t rs, int off) { return __builtin_amdgcn_raw_buffer_load_b128(rs, off, 0, 16); }
DEV v2u ldsc2(rsrc_t rs, int off) { return __builtin_amdgcn_raw_buffer_load_b64(rs, off, 0, 16); }
DEV void stsc(rsrc_t rs, int off, v4u v) { __builtin_amdgcn_raw_buffer_store_b128(v, rs, off, 0, 16); }
DEV void stsc2(rsrc_t rs, int off, v2u v) { __builtin_amdgcn_raw_buffer_store_b64(v, rs, off, 0, 16); }
DEV unsigned umax4(v4u v) { return max(max(v.x, v.y), max(v.z, v.w)); }
DEV v4u ldg(const void* p) { return __builtin_nontemporal_load((const GAS v4u*)p); }
template <int IMM> DEV void dmas(const void* base, unsigned voff, int ldst) {
    asm volatile("s_mov_b32 m0, %2\n\ts_nop 0\n\tglobal_load_lds_dwordx4 %0, %1 offset:%3 nt" :: "v"(voff), "s"(base), "s"(ldst), "i"(IMM) : "memory");
}
template <int N> DEV void wait_vm() { asm volatile("s_waitcnt vmcnt(%0)" :: "i"(N) : "memory"); }
DEV void wait_lgkm() { asm volatile("s_waitcnt lgkmcnt(0)" ::: "memory"); }
DEV v4u ldgo(const void* base, unsigned off) { return __builtin_nontemporal_load((const GAS v4u*)((const GAS char*)base + off)); }
DEV v2u trread(LAS unsigned char* p) { return __builtin_bit_cast(v2u, __builtin_amdgcn_ds_read_tr16_b64_v4i16((LAS s16x4*)p)); }
DEV v4u lds128(LAS unsigned char* p) { return *(LAS v4u*)p; }
constexpr int n_issued(int i) { return i < 0 ? 0 : ((i + (i < A_SLOWN ? A_W0 : RS)) < 16 ? (i + (i < A_SLOWN ? A_W0 : RS)) : 16); }
template <int V> struct IC { static constexpr int value = V; };
template <int B, int E, class F> DEV void static_for(F&& f) { if constexpr (B < E) { f(IC<B>{}); static_for<B + 1, E>(f); } }
#define COMPILER_FENCE() asm volatile("" ::: "memory")
#define WAIT_VM(n) __builtin_amdgcn_s_waitcnt(0x0F70 | ((n) & 15) | (((n) >> 4) << 14))
#define DRAIN() asm volatile("s_waitcnt vmcnt(0)" ::: "memory")
DEV void bar() { COMPILER_FENCE(); __builtin_amdgcn_s_waitcnt(0xC07F); __builtin_amdgcn_s_barrier(); COMPILER_FENCE(); }
#define PIN() __builtin_amdgcn_sched_barrier(0)

__global__ void __launch_bounds__(512, 2)
mla_decode_kernel(const u16* __restrict__ x, const u16* __restrict__ ckv, const u16* __restrict__ wq, const u16* __restrict__ wuk,
                  const u16* __restrict__ wuv, const u16* __restrict__ wo, const u16* __restrict__ w1, const u16* __restrict__ b1,
                  const u16* __restrict__ w2, const u16* __restrict__ b2, u16* __restrict__ out, unsigned char* ws) {
    extern __shared__ __attribute__((aligned(16))) unsigned char smem[];
    LAS unsigned char* L = (LAS unsigned char*)smem;
    const int tid = threadIdx.x, lane = tid & 63;
    {
        u64 pc;
        const unsigned line = tid >> 1;
        asm volatile("s_getpc_b64 %0" : "=s"(pc));
        asm volatile("s_mov_b32 m0, %2\n\ts_nop 0\n\tglobal_load_lds_dword %0, %1" :: "v"((line < CODE_LINES ? line : 0u) * 128u), "s"(pc), "i"(LDS_BYTES - 256) : "memory");
    }
    const u64 t_start = __builtin_amdgcn_s_memrealtime();
    asm volatile("" :: "s"(x), "s"(ckv), "s"(wq), "s"(wuk), "s"(wuv), "s"(wo), "s"(w1), "s"(b1), "s"(w2), "s"(b2), "s"(out), "s"(ws));
    u64 epoch = *(const __attribute__((address_space(4))) u64*)(ws + WS_E);
    const int w = __builtin_amdgcn_readfirstlane(tid >> 6);
    const int b = blockIdx.x, xc = b & 7, ii = b >> 3;
    const int n16 = lane & 15, g4 = lane >> 4;
    const rsrc_t rs = __builtin_amdgcn_make_buffer_rsrc(ws, 0, WS_END, 0x00020000);
    const v4u SV = {SENT, SENT, SENT, SENT};
    const float L2E = 1.4426950408889634f;
    u64 amp_t = t_start;

    if (__builtin_expect(ii < 16, 1)) {
        const int hk = 2 * xc + (ii >> 3), e = ii & 7;
        const int hf = ii, ef = xc;
        const bool has_mlp = ii < 14;
        const int fb = 14 * xc + ii;
        const int wq7 = w < 7 ? w : 6;
        unsigned tag_lo, tag_hi;
        if (w < 4) {
            __builtin_amdgcn_s_setprio(3);
            const v4u xv = ldgo(x, (lane < 56 ? lane : 0) * 16);
            v4u qa[7];
            const unsigned oq = (224 * (ef & 1) + 56 * w + (lane >> 3)) * 8192 + hf * 512 + (ef >> 1) * 128 + (lane & 7) * 16;
#pragma unroll
            for (int u = 0; u < 7; ++u) qa[u] = ldgo(wq, oq + u * 8 * 8192);
            PIN();
            __builtin_amdgcn_s_barrier();
            PIN();
            STAMP(12);
            float xf[8] = {bflo(xv.x), bfhi(xv.x), bflo(xv.y), bfhi(xv.y), bflo(xv.z), bfhi(xv.z), bflo(xv.w), bfhi(xv.w)};
            float s = ((xf[0] + xf[1]) + (xf[2] + xf[3])) + ((xf[4] + xf[5]) + (xf[6] + xf[7]));
            if (lane >= 56) s = 0.f;
            const float mean = wave_sum(s) * (1.f / 448.f);
            float s2 = 0.f;
#pragma unroll
            for (int j = 0; j < 8; ++j) { float d = xf[j] - mean; s2 += d * d; }
            if (lane >= 56) s2 = 0.f;
            const float var = wave_sum(s2) * (1.f / 448.f);
            const float mb = rbf(mean);
            const float den = rbf(__builtin_amdgcn_sqrtf(rbf(rbf(var) + 1e-5f)));
            const float rden = __builtin_amdgcn_rcpf(den);
            if (lane < 56) {
                f32x4 o0, o1;
#pragma unroll
                for (int j = 0; j < 4; ++j) { o0[j] = rbf(rbf(xf[j] - mb) * rden); o1[j] = rbf(rbf(xf[4 + j] - mb) * rden); }
                *(LAS f32x4*)(L + LW_XN + lane * 32) = o0;
                *(LAS f32x4*)(L + LW_XN + lane * 32 + 16) = o1;
            }
            COMPILER_FENCE();
            float xm[7];
#pragma unroll
            for (int u = 0; u < 7; ++u) xm[u] = *(LAS float*)(L + LW_XN + (224 * (ef & 1) + 56 * w + 8 * u + (lane >> 3)) * 4);
            asm volatile("" : "+s"(epoch));
            const u64 tag = epoch * 6364136223846793005ull + 1442695040888963407ull;
            tag_lo = (unsigned)tag; tag_hi = (unsigned)(tag >> 32);
            PIN();
            STAMP(13);
            float acc[8];
#pragma unroll
            for (int j = 0; j < 8; ++j) acc[j] = 0.f;
#pragma unroll
            for (int u = 0; u < 7; ++u) {
                acc[0] += xm[u] * bflo(qa[u].x); acc[1] += xm[u] * bfhi(qa[u].x); acc[2] += xm[u] * bflo(qa[u].y); acc[3] += xm[u] * bfhi(qa[u].y);
                acc[4] += xm[u] * bflo(qa[u].z); acc[5] += xm[u] * bfhi(qa[u].z); acc[6] += xm[u] * bflo(qa[u].w); acc[7] += xm[u] * bfhi(qa[u].w);
                PIN();
            }
            float r0 = add32(acc[0], acc[4]), r1 = add32(acc[1], acc[5]), r2 = add32(acc[2], acc[6]), r3 = add32(acc[3], acc[7]);
            float s0 = add16(r0, r2), s1 = add16(r1, r3);
            s0 += dpp<DPP_ROR8>(s0); s1 += dpp<DPP_ROR8>(s1);
            if ((lane & 8) == 0) {
                f32x2 o = {s0, s1};
                *(LAS f32x2*)(L + LW_QP + w * 256 + (8 * (lane & 7) + 4 * (lane >> 5) + 2 * ((lane >> 4) & 1)) * 4) = o;
            }
            STAMP(1);
            wait_lgkm();
            if (lane == 0) __hip_atomic_fetch_add((LAS unsigned*)(L + LW_CNT), 1u, __ATOMIC_RELAXED, __HIP_MEMORY_SCOPE_WORKGROUP);
            const unsigned th = (tag_lo >> 8) | (tag_hi << 24);
            if (w == 0) {
                for (int spin = 0; spin < (1 << 20) && *(volatile LAS unsigned*)(L + LW_CNT) < 4u; ++spin) {}
                COMPILER_FENCE();
                f32x2 a = {0.f, 0.f};
#pragma unroll
                for (int v = 0; v < 4; ++v) a += *(LAS f32x2*)(L + LW_QP + v * 256 + (lane & 31) * 8);
                const float ax = a.x, ay = a.y;
                v4u gr = {(f2u(ax) >> 8) | (tag_lo << 24), th, (f2u(ay) >> 8) | (tag_lo << 24), th};
                stsc(rs, lane < 32 ? WS_Q1 + hf * 4096 + ef * 512 + lane * 16 : OOB, gr);
                STAMP(2);
            }
            __builtin_amdgcn_s_sleep(H1_SLEEP);
            {
                const int off = WS_Q1 + hf * 4096 + w * 1024 + lane * 8;
                v2u v0, v1;
                v2u fl = ldsc2(rs, WS_FLAG + (w * 32 + (lane & 31)) * 8);
                for (int spin = 0;; ++spin) {
                    v0 = ldsc2(rs, off); v1 = ldsc2(rs, off + 512);
                    const bool ok = ((v0.x ^ (tag_lo << 24)) | (v1.x ^ (tag_lo << 24))) < 0x1000000u && v0.y == th && v1.y == th;
                    if (__all(ok) || spin > SPIN_CAP) break;
                    COMPILER_FENCE();
                }
                for (int spin = 0; !__all(fl.x == tag_lo && fl.y == tag_hi) && spin < SPIN_CAP; ++spin) {
                    __builtin_amdgcn_s_sleep(8);
                    fl = ldsc2(rs, WS_FLAG + (w * 32 + (lane & 31)) * 8);
                    COMPILER_FENCE();
                }
                const float qv = u2f(v0.x << 8) + u2f(v1.x << 8);
                *(LAS u16*)(L + LW_QV + (w * 64 + lane) * 2) = (u16)pk2(qv, qv);
            }
            STAMP(3);
            __builtin_amdgcn_s_setprio(0);
            bar();
        } else {
            const int kw = w - 4;
            if (kw == 0) { const v2u z = {0u, 0u}; *(LAS v2u*)(L + LW_Z + lane * 8) = z; *(LAS unsigned*)(L + LW_CNT) = 0u; }
            PIN();
            bar();
            v4u wk[16];
            const unsigned ok = (128 * ef + 32 * kw + (n16 >> 2)) * 8192 + hf * 512 + g4 * 64 + (n16 & 3) * 16;
#pragma unroll
            for (int u = 0; u < 16; ++u) wk[u] = ldgo(wuk, ok + (u >> 1) * 4 * 8192 + (u & 1) * 256);
            PIN();
            asm volatile("" : "+s"(epoch));
            const u64 tag = epoch * 6364136223846793005ull + 1442695040888963407ull;
            tag_lo = (unsigned)tag; tag_hi = (unsigned)(tag >> 32);
            STAMP(1);
            bar();
            v4u qf[8];
#pragma unroll
            for (int u = 0; u < 8; ++u) qf[u] = lds128(L + ((n16 >> 2) == (u >> 1) ? LW_QV : LW_Z) + (u & 1) * 256 + g4 * 64 + (n16 & 3) * 16);
            PIN();
            f32x4 d0 = {0.f, 0.f, 0.f, 0.f}, d1 = {0.f, 0.f, 0.f, 0.f};
#pragma unroll
            for (int u = 0; u < 8; ++u) d0 = mfma(wk[u], qf[u], d0);
#pragma unroll
            for (int u = 0; u < 8; ++u) d1 = mfma(wk[8 + u], qf[u], d1);
            float v0 = (lane & 2) ? ((lane & 1) ? d0.w : d0.z) : ((lane & 1) ? d0.y : d0.x);
            float v1 = (lane & 2) ? ((lane & 1) ? d1.w : d1.z) : ((lane & 1) ? d1.y : d1.x);
            v0 += dpp<DPP_X1>(v0); v1 += dpp<DPP_X1>(v1); v0 += dpp<DPP_X2>(v0); v1 += dpp<DPP_X2>(v1);
            const float v = ((lane & 1) ? v1 : v0) * 0.0625f;
            unsigned vb = pk2(v, v) & 0xffffu;
            vb = vb < 0xfffeu ? vb : 0xfffeu;
            if ((lane & 2) == 0) *(LAS u16*)(L + LW_QA + (32 * kw + 16 * (lane & 1) + (n16 & 12) + g4) * 2) = (u16)vb;
            STAMP(3);
        }
        const u64 tag = ((u64)tag_hi << 32) | tag_lo;
        bar();
        if (w == 4) stsc(rs, lane < 16 ? WS_Q2 + hf * 2048 + ef * 256 + lane * 16 : OOB, lds128(L + LW_QA + n16 * 16));
        PIN();
        STAMP(4);
        {
            const unsigned vo = (512 * hk + 64 * w + g4) * 2048 + e * 256;
#pragma unroll
            for (int u = 0; u < 16; ++u) {
                if (u >= COLS_WIN) wait_vm<COLS_WIN - 1>();
                dmas<0>(ckv, vo + u * 8192 + ((n16 ^ ((g4 << 2) | (u & 3))) * 16), LW_COLS + (64 * w + 4 * u) * 256);
            }
        }
        PIN();
        STAMP(5);
        wait_vm<0>();
        bar();
        STAMP(6);
        v4u vf[16];
        {
            const int q = n16 >> 2, p = lane & 3;
#pragma unroll
            for (int t = 0; t < 2; ++t) {
                LAS unsigned char* base = L + LW_COLS + (8 * g4 + 4 * t + q) * 256 + (((2 * w + (p >> 1)) ^ ((q << 2) | ((2 * g4 + t) & 3))) * 16) + 8 * (p & 1);
#pragma unroll
                for (int ks = 0; ks < 16; ++ks) {
                    v2u r = trread(base + ks * 8192);
                    if (t == 0) { vf[ks].x = r.x; vf[ks].y = r.y; } else { vf[ks].z = r.x; vf[ks].w = r.y; }
                }
            }
        }
        PIN();
        const int off3 = WS_S3 + (hk * 32 + (lane >> 2)) * 512 + w * 64 + (lane & 3) * 16;
        if (w == 0) {
            for (int spin = 0; spin < SPIN_CAP; ++spin) {
                const unsigned hv = __builtin_amdgcn_raw_buffer_load_b32(rs, WS_H3 + (hk * 8 + (lane & 7)) * 128, 0, 16);
                if (__all(hv != SENT)) break;
                COMPILER_FENCE();
            }
            if (H3_SLEEP) __builtin_amdgcn_s_sleep(H3_SLEEP);
        }
        STAMP(14);
        bar();
        v4u sc0 = ldsc(rs, off3), sc1 = ldsc(rs, off3 + 16 * 512);
        for (int spin = 0; spin < SPIN_CAP; ++spin) {
            const bool ok0 = __all(umax4(sc0) != SENT), ok1 = __all(umax4(sc1) != SENT);
            if (ok0 && ok1) break;
            if (!ok0) sc0 = ldsc(rs, off3);
            if (!ok1) sc1 = ldsc(rs, off3 + 16 * 512);
            COMPILER_FENCE();
        }
        STAMP(7);
        float sm_m, sm_l;
        {
            float s[16] = {bflo(sc0.x), bfhi(sc0.x), bflo(sc0.y), bfhi(sc0.y), bflo(sc0.z), bfhi(sc0.z), bflo(sc0.w), bfhi(sc0.w),
                           bflo(sc1.x), bfhi(sc1.x), bflo(sc1.y), bfhi(sc1.y), bflo(sc1.z), bfhi(sc1.z), bflo(sc1.w), bfhi(sc1.w)};
            float m = s[0];
#pragma unroll
            for (int j = 1; j < 16; ++j) m = fmaxf(m, s[j]);
            m = fmaxf(m, dpp<DPP_X1>(m)); m = fmaxf(m, dpp<DPP_ROR4>(m)); m = fmaxf(m, dpp<DPP_ROR8>(m));
            m = max16(m); m = max32(m);
            const float mm = -m * L2E;
#pragma unroll
            for (int j = 0; j < 16; ++j) s[j] = __builtin_amdgcn_exp2f(__builtin_fmaf(s[j], L2E, mm));
            v4u p0 = {pk2(s[0], s[1]), pk2(s[2], s[3]), pk2(s[4], s[5]), pk2(s[6], s[7])};
            v4u p1 = {pk2(s[8], s[9]), pk2(s[10], s[11]), pk2(s[12], s[13]), pk2(s[14], s[15])};
            const int tile = lane >> 2, head = 2 * w + ((lane >> 1) & 1), gp = 2 * (tile & 1) + (lane & 1);
            LAS unsigned char* dst = L + LW_PF + (tile >> 1) * 1024 + head * 64 + ((gp ^ ((-(head >> 2)) & 3)) * 16);
            *(LAS v4u*)dst = p0;
            *(LAS v4u*)(dst + 8192) = p1;
            PIN();
            sm_m = m;
            sm_l = (((s[0] + s[1]) + (s[2] + s[3])) + ((s[4] + s[5]) + (s[6] + s[7]))) + (((s[8] + s[9]) + (s[10] + s[11])) + ((s[12] + s[13]) + (s[14] + s[15])));
        }
        bar();
        v4u m1[2], m2[2];
        unsigned b1v = 0;
        {
            v4u pf[16];
            LAS unsigned char* pb = L + LW_PF + n16 * 64 + ((g4 ^ ((-(n16 >> 2)) & 3)) * 16);
#pragma unroll
            for (int ks = 0; ks < 16; ++ks) pf[ks] = lds128(pb + ks * 1024);
            PIN();
            f32x4 d = {0.f, 0.f, 0.f, 0.f};
#pragma unroll
            for (int ks = 0; ks < 16; ++ks) d = mfma(vf[ks], pf[ks], d);
            float l = sm_l;
            l += dpp<DPP_X1>(l); l += dpp<DPP_ROR4>(l); l += dpp<DPP_ROR8>(l); l = add16(l, l); l = add32(l, l);
            if ((lane & 61) == 0) { v2u o = {nosent(f2u(sm_m)), nosent(f2u(l))}; *(LAS v2u*)(L + LW_ST + (2 * w + (lane >> 1)) * 8) = o; }
            v2u o = {nosent(pk2(d.x, d.y)), nosent(pk2(d.z, d.w))};
            *(LAS v2u*)(L + LW_TR + n16 * 272 + w * 32 + g4 * 8) = o;
        }
        bar();
        if (w < 4) stsc(rs, WS_P4 + ((4 * w + g4) * 16 + hk) * 2048 + e * 256 + n16 * 16, lds128(L + LW_TR + (4 * w + g4) * 272 + n16 * 16));
        else if (w == 4 && e == 0) { if (lane < 8) stsc(rs, WS_ST4 + hk * 128 + lane * 16, lds128(L + LW_ST + lane * 16)); }
        PIN();
        STAMP(8);
        if (has_mlp) {
            {
                const char* p1 = (const char*)w1 + (size_t)(64 * wq7 + (lane >> 1)) * 3584 + fb * 32 + (lane & 1) * 16;
                m1[0] = ldg(p1); m1[1] = ldg(p1 + 32 * 3584);
                const char* p2 = (const char*)w2 + (size_t)(fb * 16 + 2 * w) * 896 + (lane < 56 ? lane : 0) * 16;
                m2[0] = ldg(p2); m2[1] = ldg(p2 + 896);
                b1v = *(const GAS unsigned*)((const char*)b1 + fb * 32 + w * 4);
            }
            PIN();
            if (w < 7) {
                float acc[8];
#pragma unroll
                for (int j = 0; j < 8; ++j) acc[j] = 0.f;
#pragma unroll
                for (int u = 0; u < 2; ++u) {
                    const float xm = *(LAS float*)(L + LW_XN + (64 * w + 32 * u + (lane >> 1)) * 4);
                    acc[0] += xm * bflo(m1[u].x); acc[1] += xm * bfhi(m1[u].x); acc[2] += xm * bflo(m1[u].y); acc[3] += xm * bfhi(m1[u].y);
                    acc[4] += xm * bflo(m1[u].z); acc[5] += xm * bfhi(m1[u].z); acc[6] += xm * bflo(m1[u].w); acc[7] += xm * bfhi(m1[u].w);
                }
                float r0 = add32(acc[0], acc[4]), r1 = add32(acc[1], acc[5]), r2 = add32(acc[2], acc[6]), r3 = add32(acc[3], acc[7]);
                float s0 = add16(r0, r2), s1 = add16(r1, r3);
                s0 += dpp<DPP_X2>(s0); s1 += dpp<DPP_X2>(s1);
                s0 += dpp<DPP_ROR4>(s0); s1 += dpp<DPP_ROR4>(s1); s0 += dpp<DPP_ROR8>(s0); s1 += dpp<DPP_ROR8>(s1);
                if ((lane & 14) == 0) {
                    f32x2 o = {s0, s1};
                    *(LAS f32x2*)(L + LW_HP + w * 64 + (8 * (lane & 1) + 4 * (lane >> 5) + 2 * ((lane >> 4) & 1)) * 4) = o;
                }
            }
            bar();
            f32x2 hh = {0.f, 0.f};
#pragma unroll
            for (int v = 0; v < 7; ++v) hh += *(LAS f32x2*)(L + LW_HP + v * 64 + w * 8);
            float h0 = rbf(rbf(hh.x) + bflo(b1v)), h1 = rbf(rbf(hh.y) + bfhi(b1v));
            h0 = h0 > 0.f ? h0 : 0.f; h1 = h1 > 0.f ? h1 : 0.f;
            h0 = rbf(h0 * h0); h1 = rbf(h1 * h1);
            if (lane < 56) {
                f32x4 o0 = {h0 * bflo(m2[0].x) + h1 * bflo(m2[1].x), h0 * bfhi(m2[0].x) + h1 * bfhi(m2[1].x),
                            h0 * bflo(m2[0].y) + h1 * bflo(m2[1].y), h0 * bfhi(m2[0].y) + h1 * bfhi(m2[1].y)};
                f32x4 o1 = {h0 * bflo(m2[0].z) + h1 * bflo(m2[1].z), h0 * bfhi(m2[0].z) + h1 * bfhi(m2[1].z),
                            h0 * bflo(m2[0].w) + h1 * bflo(m2[1].w), h0 * bfhi(m2[0].w) + h1 * bfhi(m2[1].w)};
                *(LAS f32x4*)(L + LW_COLS + w * 1792 + lane * 32) = o0;
                *(LAS f32x4*)(L + LW_COLS + w * 1792 + lane * 32 + 16) = o1;
            }
            bar();
            if (tid < 112) {
                f32x4 a = {0.f, 0.f, 0.f, 0.f};
#pragma unroll
                for (int v = 0; v < 8; ++v) a += *(LAS f32x4*)(L + LW_COLS + v * 1792 + tid * 16);
                float a0 = a.x, a1 = a.y, a2 = a.z, a3 = a.w;
                v4u o = {nosent(f2u(a0)), nosent(f2u(a1)), nosent(f2u(a2)), nosent(f2u(a3))};
                stsc(rs, WS_M5 + (tid >> 3) * (112 * 128) + fb * 128 + (tid & 7) * 16, o);
            }
            STAMP(9);
        } else {
            const int fi = 2 * xc + (ii - 14);
            if (fi < 14) {
                const unsigned xb = *(const GAS u16*)(x + fi * 32 + (lane & 31)), b2b = *(const GAS u16*)(b2 + fi * 32 + (lane & 31));
                const int offm = WS_M5 + fi * (112 * 128) + wq7 * 2048 + lane * 16, offs = WS_P5 + fi * 16384 + w * 2048 + lane * 16;
                v4u a0 = ldsc(rs, offm), a1 = ldsc(rs, offm + 1024), c0 = ldsc(rs, offs), c1 = ldsc(rs, offs + 1024);
                for (int spin = 0; spin < SPIN_CAP; ++spin) {
                    const bool k0 = __all(umax4(a0) != SENT), k1 = __all(umax4(a1) != SENT), k2 = __all(umax4(c0) != SENT), k3 = __all(umax4(c1) != SENT);
                    if (k0 && k1 && k2 && k3) break;
                    if (!k0) a0 = ldsc(rs, offm);
                    if (!k1) a1 = ldsc(rs, offm + 1024);
                    if (!k2) c0 = ldsc(rs, offs);
                    if (!k3) c1 = ldsc(rs, offs + 1024);
                    COMPILER_FENCE();
                }
                STAMP(10);
                {
                    const f32x4 am = __builtin_bit_cast(f32x4, a0) + __builtin_bit_cast(f32x4, a1);
                    const f32x4 as = __builtin_bit_cast(f32x4, c0) + __builtin_bit_cast(f32x4, c1);
                    float s0 = add16(add32(am.x, am.z), add32(am.y, am.w)), s1 = add16(add32(as.x, as.z), add32(as.y, as.w));
                    s0 += dpp<DPP_ROR8>(s0); s1 += dpp<DPP_ROR8>(s1);
                    f32x2 o = {s0, s1};
                    if ((lane & 8) == 0) *(LAS f32x2*)(L + LW_FR + w * 256 + ((lane & 7) * 4 + 2 * (lane >> 5) + ((lane >> 4) & 1)) * 8) = o;
                }
                bar();
                if (tid < 32) {
                    f32x2 a = {0.f, 0.f};
#pragma unroll
                    for (int v = 0; v < 7; ++v) a += *(LAS f32x2*)(L + LW_FR + v * 256 + tid * 8);
                    const float sv = a.y + *(LAS float*)(L + LW_FR + 7 * 256 + tid * 8 + 4);
                    const float o2 = rbf(rbf(a.x) + bflo(b2b));
                    const float gate = rbf(o2 * __builtin_amdgcn_rcpf(1.0f + __builtin_amdgcn_exp2f(-o2 * L2E)));
                    const float o = bflo(xb) + rbf(rbf(sv) * gate);
                    *(GAS u16*)(out + fi * 32 + tid) = (u16)pk2(o, o);
                    if (fi == 0 && tid == 0) *(GAS u64*)(ws + WS_E) = tag;
                }
                STAMP(11);
            }
        }
    } else {
        const int ia = ii - 16, ai = xc * 16 + ia;
        const int hd = 2 * xc + (ia >> 3), ch = (ia >> 2) & 1, dq = ia & 3;
        if (w < 4) {
            v4u a[32], q[16];
            const int r8 = lane >> 3, pos = lane & 7;
            const unsigned vo0 = (64 * ai + 16 * w + r8) * 2048 + ((pos ^ (r8 >> 1)) * 16);
            const unsigned vo1 = (64 * ai + 16 * w + 8 + r8) * 2048 + ((pos ^ (4 + (r8 >> 1))) * 16);
            const int ring = LA_RING + w * RS * 2048;
            LAS unsigned char* ra0 = L + ring + n16 * 128 + ((g4 ^ (n16 >> 1)) * 16);
            LAS unsigned char* ra1 = L + ring + n16 * 128 + (((4 + g4) ^ (n16 >> 1)) * 16);
            LAS unsigned char* qb = L + LA_Q + n16 * 64 + ((g4 ^ ((-(n16 >> 2)) & 3)) * 16);
            f32x4 d = {0.f, 0.f, 0.f, 0.f};
            static_for<0, 16>([&](auto I) {
                constexpr int i = I.value;
                static_for<n_issued(i - 1), n_issued(i)>([&](auto J) {
                    constexpr int j = J.value;
                    dmas<j * 128>(ckv, vo0, ring + (j % RS) * 2048 - j * 128);
                    dmas<j * 128>(ckv, vo1, ring + (j % RS) * 2048 + 1024 - j * 128);
                });
                if constexpr (n_issued(i) == 16 && n_issued(i - 1) < 16) {
                    STAMP(1);
                    bar();
                    STAMP(2);
#pragma unroll
                    for (int j = 0; j < 16; ++j) q[j] = lds128(qb + j * 1024);
                    PIN();
#pragma unroll
                    for (int j = 0; j < 16; ++j) d = mfma(a[j], q[j], d);
                    PIN();
#pragma unroll
                    for (int j = 0; j < 16; ++j) q[j] = lds128(qb + (16 + j) * 1024);
                    PIN();
#pragma unroll
                    for (int j = 16; j < 2 * i; ++j) d = mfma(a[j], q[j - 16], d);
                    PIN();
                }
                wait_vm<2 * (n_issued(i) - i - 1)>();
                a[2 * i] = lds128(ra0 + (i % RS) * 2048);
                a[2 * i + 1] = lds128(ra1 + (i % RS) * 2048);
                if constexpr (n_issued(i) < 16) {
                    asm volatile("s_waitcnt lgkmcnt(0)" : "+v"(a[2 * i]), "+v"(a[2 * i + 1]) :: "memory");
                } else {
                    d = mfma(a[2 * i], q[2 * i - 16], d);
                    d = mfma(a[2 * i + 1], q[2 * i + 1 - 16], d);
                    PIN();
                }
            });
            unsigned a0 = nosent(pk2(d.x, d.y)), a1 = nosent(pk2(d.z, d.w)), c0 = a0, c1 = a1;
            swap16(a0, c0); swap16(a1, c1);
            v4u o = {a0, a1, c0, c1};
            stsc(rs, (g4 & 1) == 0 ? WS_S3 + (ai * 4 + w) * 512 + n16 * 32 + (g4 >> 1) * 16 : OOB, o);
            PIN();
            STAMP(3);
        } else {
            const int tw = w - 4;
            if (tw == 0) {
#pragma unroll
                for (int u = 0; u < 10; ++u) stsc(rs, WS_Q2 + ai * 10240 + u * 1024 + lane * 16, SV);
            }
            PIN();
            asm volatile("" : "+s"(epoch));
            const u64 tag = epoch * 6364136223846793005ull + 1442695040888963407ull;
            const unsigned tag_lo = (unsigned)tag, tag_hi = (unsigned)(tag >> 32);
            if (tw == 0) {
                wait_vm<0>();
                v2u t = {tag_lo, tag_hi};
                stsc2(rs, lane == 0 ? WS_FLAG + ai * 8 : OOB, t);
            }
            STAMP(1);
            while (__builtin_amdgcn_s_memrealtime() - t_start < FLAG_TICKS) __builtin_amdgcn_s_sleep(2);
            for (int spin = 0;; ++spin) {
                v4u v = ldsc(rs, WS_FLAG + lane * 16);
                const bool ok = v.x == tag_lo && v.y == tag_hi && v.z == tag_lo && v.w == tag_hi;
                if (__all(ok) || spin > SPIN_CAP) break;
                __builtin_amdgcn_s_sleep(8);
                COMPILER_FENCE();
            }
            STAMP(2);
            while (__builtin_amdgcn_s_memrealtime() - t_start < H2_TICKS) __builtin_amdgcn_s_sleep(1);
            for (int spin = 0; spin < SPIN_CAP; ++spin) {
                v4u v = ldsc(rs, WS_Q2 + tw * 8192 + (ia & 7) * 1024 + lane * 16);
                if (__all(umax4(v) != SENT)) break;
                COMPILER_FENCE();
            }
            STAMP(12);
            {
                v4u qv[8];
                const int off = WS_Q2 + tw * 8192 + lane * 16;
#pragma unroll
                for (int u = 0; u < 8; ++u) qv[u] = ldsc(rs, off + u * 1024);
                for (int spin = 0; spin < SPIN_CAP; ++spin) {
                    bool all = true;
#pragma unroll
                    for (int u = 0; u < 8; ++u) {
                        if (!__all(umax4(qv[u]) != SENT)) { all = false; qv[u] = ldsc(rs, off + u * 1024); }
                    }
                    if (all) break;
                    COMPILER_FENCE();
                }
                LAS unsigned char* dst = L + LA_Q + (lane >> 2) * 1024 + tw * 256 + (((lane & 3) ^ ((-tw) & 3)) * 16);
#pragma unroll
                for (int u = 0; u < 8; ++u) *(LAS v4u*)(dst + (u & 1) * 16384 + (u >> 1) * 64) = qv[u];
            }
            STAMP(3);
            bar();
            if (tw == 0) __builtin_amdgcn_raw_buffer_store_b32(0u, rs, lane == 0 ? WS_H3 + ai * 128 : OOB, 0, 16);
            {
                const int r8 = lane >> 3, pos = lane & 7;
                const unsigned vo = (512 * ch + 128 * tw + r8) * 8192 + hd * 512 + dq * 128;
#pragma unroll
                for (int u = 0; u < TAIL_X; ++u)
                    dmas<0>(wuv, vo + u * 65536 + ((pos ^ ((((r8 >> 1) & 1) | ((u & 1) << 1)) << 1)) * 16), LA_UV + (128 * tw + 8 * u) * 128);
            }
            STAMP(4);
        }
        bar();
        {
            const int r8 = lane >> 3, pos = lane & 7, tq = w & 3, th = w >> 2;
            const unsigned vo = (512 * ch + 128 * tq + r8) * 8192 + hd * 512 + dq * 128;
#pragma unroll
            for (int v = 0; v < (16 - TAIL_X) / 2; ++v) {
                const int u = TAIL_X + 2 * v + th;
                dmas<0>(wuv, vo + u * 65536 + ((pos ^ ((((r8 >> 1) & 1) | ((u & 1) << 1)) << 1)) * 16), LA_UV + (128 * tq + 8 * u) * 128);
            }
            const unsigned so = (hd * 256 + dq * 64 + 8 * w) * 896 + lane * 16;
            if (lane < 56) {
#pragma unroll
                for (int u = 0; u < 8; ++u) dmas<0>(wo, so + u * 896, LA_WO + (8 * w + u) * 928 + w * 128);
            }
        }
        PIN();
        wait_vm<0>();
        STAMP(5);
        bar();
        const int off4 = WS_P4 + (hd * 16 + (lane >> 3)) * 2048 + ch * 1024 + w * 128 + (lane & 7) * 16;
        const int soff = WS_ST4 + g4 * 512 + hd * 8;
        v2u s0 = ldsc2(rs, soff), s1 = ldsc2(rs, soff + 128), s2 = ldsc2(rs, soff + 256), s3 = ldsc2(rs, soff + 384);
        v4u c0 = ldsc(rs, off4), c1 = ldsc(rs, off4 + 8 * 2048);
        PIN();
        v4u uf[8], of[8];
        {
            const int q = n16 >> 2, p = lane & 3;
#pragma unroll
            for (int t = 0; t < 2; ++t) {
                const int sw = ((q >> 1) | ((g4 & 1) << 1)) << 1;
                LAS unsigned char* ub = L + LA_UV + (64 * w + 8 * g4 + 4 * t + q) * 128 + 8 * (p & 1);
                const int wq7 = w < 7 ? w : 6;
                LAS unsigned char* ob = L + LA_WO + (8 * g4 + 4 * t + q) * 928 + g4 * 128 + wq7 * 128 + p * 8;
#pragma unroll
                for (int ks = 0; ks < 2; ++ks)
#pragma unroll
                    for (int ct = 0; ct < 4; ++ct) {
                        v2u r = trread(ub + ks * 4096 + (((2 * ct + (p >> 1)) ^ sw) * 16));
                        v2u o = trread(ob + ks * (32 * 928 + 512) + ct * 32);
                        if (t == 0) { uf[ks * 4 + ct].x = r.x; uf[ks * 4 + ct].y = r.y; of[ks * 4 + ct].x = o.x; of[ks * 4 + ct].y = o.y; }
                        else { uf[ks * 4 + ct].z = r.x; uf[ks * 4 + ct].w = r.y; of[ks * 4 + ct].z = o.x; of[ks * 4 + ct].w = o.y; }
                    }
            }
        }
        PIN();
        STAMP(6);
        {
            for (int spin = 0; spin < SPIN_CAP; ++spin) {
                const bool ks = __all(max(max(max(s0.x, s0.y), max(s1.x, s1.y)), max(max(s2.x, s2.y), max(s3.x, s3.y))) != SENT);
                const bool kc = __all(max(umax4(c0), umax4(c1)) != SENT);
                if (ks && kc) break;
                if (!ks) { s0 = ldsc2(rs, soff); s1 = ldsc2(rs, soff + 128); s2 = ldsc2(rs, soff + 256); s3 = ldsc2(rs, soff + 384); }
                if (!kc) { c0 = ldsc(rs, off4); c1 = ldsc(rs, off4 + 8 * 2048); }
                COMPILER_FENCE();
            }
            STAMP(7);
            {
                LAS unsigned char* bn = L + LA_BN + w * 2304;
                *(LAS v4u*)(bn + (lane >> 3) * 144 + (lane & 7) * 16) = c0;
                *(LAS v4u*)(bn + (8 + (lane >> 3)) * 144 + (lane & 7) * 16) = c1;
                COMPILER_FENCE();
                c0 = lds128(bn + n16 * 144 + g4 * 16);
                c1 = lds128(bn + n16 * 144 + 64 + g4 * 16);
            }
            f32x4 d[4];
#pragma unroll
            for (int ct = 0; ct < 4; ++ct) {
                f32x4 z = {0.f, 0.f, 0.f, 0.f};
                d[ct] = mfma(c0, uf[ct], z);
            }
#pragma unroll
            for (int ct = 0; ct < 4; ++ct) d[ct] = mfma(c1, uf[4 + ct], d[ct]);
            const float m0 = u2f(s0.x), m1 = u2f(s1.x), m2 = u2f(s2.x), m3 = u2f(s3.x);
            float M = fmaxf(fmaxf(m0, m1), fmaxf(m2, m3));
            M = max16(M); M = max32(M);
            float f0 = __builtin_amdgcn_exp2f((m0 - M) * L2E), f1 = __builtin_amdgcn_exp2f((m1 - M) * L2E);
            float f2 = __builtin_amdgcn_exp2f((m2 - M) * L2E), f3 = __builtin_amdgcn_exp2f((m3 - M) * L2E);
            float Ls = (f0 * u2f(s0.y) + f1 * u2f(s1.y)) + (f2 * u2f(s2.y) + f3 * u2f(s3.y));
            Ls = add16(Ls, Ls); Ls = add32(Ls, Ls);
            const float rl = __builtin_amdgcn_rcpf(Ls);
            f0 *= rl; f1 *= rl; f2 *= rl; f3 *= rl;
            float cx[4];
#pragma unroll
            for (int ct = 0; ct < 4; ++ct) cx[ct] = (f0 * d[ct].x + f1 * d[ct].y) + (f2 * d[ct].z + f3 * d[ct].w);
            const float r0 = add32(cx[0], cx[2]), r1 = add32(cx[1], cx[3]);
            const float cv = add16(r0, r1);
            const float hi = rbf(cv), lo = cv - hi;
            const unsigned pk = pk2(hi, lo);
            *(LAS u16*)(L + LA_CT + w * 144 + lane * 2) = (u16)pk;
            *(LAS u16*)(L + LA_CT + (8 + w) * 144 + lane * 2) = (u16)(pk >> 16);
        }
        bar();
        if (w < 7) {
            const v4u a0 = lds128(L + LA_CT + n16 * 144 + g4 * 16), a1 = lds128(L + LA_CT + n16 * 144 + 64 + g4 * 16);
            f32x4 d[4];
#pragma unroll
            for (int ct = 0; ct < 4; ++ct) {
                f32x4 z = {0.f, 0.f, 0.f, 0.f};
                d[ct] = mfma(a0, of[ct], z);
            }
#pragma unroll
            for (int ct = 0; ct < 4; ++ct) d[ct] = mfma(a1, of[4 + ct], d[ct]);
            float sx[4];
#pragma unroll
            for (int ct = 0; ct < 4; ++ct) sx[ct] = (d[ct].x + d[ct].y) + (d[ct].z + d[ct].w);
            const float r0 = add32(sx[0], sx[2]), r1 = add32(sx[1], sx[3]);
            const float sv = add16(r0, r1);
            const unsigned su = nosent(f2u(sv));
            v4u o;
            o.x = su;
            o.y = (unsigned)__builtin_amdgcn_update_dpp(0, (int)su, 0x55, 0xf, 0xf, true);
            o.z = (unsigned)__builtin_amdgcn_update_dpp(0, (int)su, 0xAA, 0xf, 0xf, true);
            o.w = (unsigned)__builtin_amdgcn_update_dpp(0, (int)su, 0xFF, 0xf, 0xf, true);
            stsc(rs, (lane & 3) == 0 ? WS_P5 + (2 * w + (lane >> 5)) * 16384 + ai * 128 + (lane & 31) * 4 : OOB, o);
        }
        STAMP(8);
    }
    STAMP_OUT();
}

extern "C" void kernel_launch(void* const* d_in, const int* in_sizes, int n_in, void* d_out, int out_size, void* d_ws, size_t ws_size,
                              hipStream_t stream) {
    (void)hipFuncSetAttribute((const void*)mla_decode_kernel, hipFuncAttributeMaxDynamicSharedMemorySize, LDS_BYTES);
    mla_decode_kernel<<<256, 512, LDS_BYTES, stream>>>((const u16*)d_in[0], (const u16*)d_in[1], (const u16*)d_in[2], (const u16*)d_in[3],
                                                       (const u16*)d_in[4], (const u16*)d_in[5], (const u16*)d_in[7], (const u16*)d_in[8],
                                                       (const u16*)d_in[9], (const u16*)d_in[10], (u16*)d_out, (unsigned char*)d_ws);
}
```

```cpp
#define AMP 99
#define AMPB 112
#define AMPW 0
#define AMPMODE 0
#include <hip/hip_runtime.h>
#include <hip/hip_bf16.h>

typedef unsigned short u16;
typedef unsigned long long u64;
typedef unsigned v4u __attribute__((ext_vector_type(4)));
typedef unsigned v2u __attribute__((ext_vector_type(2)));
typedef float f32x4 __attribute__((ext_vector_type(4)));
typedef float f32x2 __attribute__((ext_vector_type(2)));
typedef __bf16 bf16x8 __attribute__((ext_vector_type(8)));
typedef __bf16 bf16x2 __attribute__((ext_vector_type(2)));
typedef short s16x4 __attribute__((ext_vector_type(4)));
#define LAS __attribute__((address_space(3)))
#define GAS __attribute__((address_space(1)))
#define DEV __device__ __forceinline__
typedef __amdgpu_buffer_rsrc_t rsrc_t;

constexpr int WS_E = 0;
constexpr int WS_FLAG = 4096;
constexpr int WS_Q1 = 8192;
constexpr int WS_Q2 = WS_Q1 + 65536;
constexpr int WS_S3 = WS_Q2 + 32768;
constexpr int WS_P4 = WS_S3 + 262144;
constexpr int WS_ST4 = WS_P4 + 524288;
constexpr int WS_P5 = WS_ST4 + 4096;
constexpr int WS_M5 = WS_P5 + 14 * 16384;
constexpr int WS_H3 = WS_M5 + 14 * 112 * 128;
constexpr int WS_END = WS_Q2 + 128 * 10240;
static_assert(WS_H3 + 128 * 128 <= WS_END, "workspace map");
constexpr unsigned SENT = 0xffffffffu;
constexpr int SPIN_CAP = 1 << 14;
constexpr int OOB = 0x7ffffff0;
constexpr unsigned CODE_LINES = 145;

constexpr int LW_COLS = 0;
constexpr int LW_PF = 131072;
constexpr int LW_XN = LW_PF + 16384;
constexpr int LW_QP = LW_XN + 2048;
constexpr int LW_QV = LW_QP + 1024;
constexpr int LW_HP = LW_QV + 512;
constexpr int LW_FR = LW_HP + 512;
constexpr int LW_TR = LW_FR + 2048;
constexpr int LW_ST = LW_TR + 16 * 272;
constexpr int LW_QA = LW_ST + 128;
constexpr int LW_Z = LW_QA + 256;
constexpr int LW_CNT = LW_Z + 512;
#define RS 4
constexpr int LA_Q = 0;
constexpr int LA_RING = 32768;
constexpr int LA_WO = 0;
constexpr int LA_BN = LA_WO + 60416;
constexpr int LA_CT = LA_BN + 8 * 2304;
constexpr int LA_UV = 83968; static_assert(LA_RING + 4 * RS * 2048 <= 83968, "ring");
static_assert(LA_CT + 2304 <= LA_UV && LA_UV + 65536 <= 161280 - 256, "A LDS map");
constexpr int LDS_BYTES = 161280;

#define A_W0 1
#define A_SLOWN 2
#define EARLY_COLS 0
#define H1_SLEEP 8
#define H3_SLEEP 0
#define TAIL_X 4
#define COLS_WIN 16
#define H2_TICKS 280
#define FLAG_TICKS 150
#define STAMP(i) do { if (i == AMP) { __builtin_amdgcn_sched_barrier(0); asm volatile("s_memrealtime %0\n\ts_waitcnt lgkmcnt(0)" : "=s"(amp_t) :: "memory"); __builtin_amdgcn_sched_barrier(0); } } while (0)
#define STAMP_OUT() do { if (b == AMPB && w == AMPW) { u64 ref = t_start; if (AMPMODE == 1) { amp_t = t_start; ref = t_first; } if (AMPMODE >= 2) { u64 m = ~0ull, mx = 0; for (int k = 0; k < 4; ++k) { u64 v = __hip_atomic_load((u64*)(ws + (64u << 20)) + k * 64 + lane, __ATOMIC_RELAXED, __HIP_MEMORY_SCOPE_AGENT); m = v < m ? v : m; mx = v > mx ? v : mx; } for (int o = 32; o > 0; o >>= 1) { u64 y = __shfl_xor(m, o); m = y < m ? y : m; u64 z = __shfl_xor(mx, o); mx = z > mx ? z : mx; } ref = m; if (AMPMODE == 3) amp_t = mx; if (amp_t - ref > 5000) ref = amp_t; } const u64 n0 = __builtin_amdgcn_s_memrealtime(); const u64 d = 10 * (amp_t - ref); while (__builtin_amdgcn_s_memrealtime() - n0 < d) __builtin_amdgcn_s_sleep(1); } } while (0)

DEV unsigned f2u(float f) { return __builtin_bit_cast(unsigned, f); }
DEV float u2f(unsigned u) { return __builtin_bit_cast(float, u); }
DEV float bflo(unsigned w) { return u2f(w << 16); }
DEV float bfhi(unsigned w) { return u2f(w & 0xffff0000u); }
DEV unsigned pk2(float a, float b) { f32x2 p = {a, b}; bf16x2 r = __builtin_convertvector(p, bf16x2); return __builtin_bit_cast(unsigned, r); }
DEV float rbf(float a) { return bflo(pk2(a, a)); }
DEV unsigned nosent(unsigned u) { return u < 0xfffffffeu ? u : 0xfffffffeu; }
DEV f32x4 mfma(v4u a, v4u b, f32x4 c) {
    return __builtin_amdgcn_mfma_f32_16x16x32_bf16(__builtin_bit_cast(bf16x8, a), __builtin_bit_cast(bf16x8, b), c, 0, 0, 0);
}
template <int CTRL> DEV float dpp(float v) { return u2f((unsigned)__builtin_amdgcn_update_dpp(0, (int)f2u(v), CTRL, 0xf, 0xf, true)); }
constexpr int DPP_X1 = 0xB1, DPP_X2 = 0x4E, DPP_ROR4 = 0x124, DPP_ROR8 = 0x128;
DEV void swap16(unsigned& a, unsigned& b) { auto r = __builtin_amdgcn_permlane16_swap(a, b, false, false); a = r[0]; b = r[1]; }
DEV void swap32(unsigned& a, unsigned& b) { auto r = __builtin_amdgcn_permlane32_swap(a, b, false, false); a = r[0]; b = r[1]; }
DEV float add16(float a, float b) { unsigned x = f2u(a), y = f2u(b); swap16(x, y); return u2f(x) + u2f(y); }
DEV float add32(float a, float b) { unsigned x = f2u(a), y = f2u(b); swap32(x, y); return u2f(x) + u2f(y); }
DEV float max16(float a) { unsigned x = f2u(a), y = x; swap16(x, y); return fmaxf(u2f(x), u2f(y)); }
DEV float max32(float a) { unsigned x = f2u(a), y = x; swap32(x, y); return fmaxf(u2f(x), u2f(y)); }
DEV float wave_sum(float v) {
    v += dpp<DPP_X1>(v); v += dpp<DPP_X2>(v); v += dpp<DPP_ROR4>(v); v += dpp<DPP_ROR8>(v);
    v = add16(v, v); v = add32(v, v); return v;
}
DEV v4u ldsc(rsrc_t rs, int off) { return __builtin_amdgcn_raw_buffer_load_b128(rs, off, 0, 16); }
DEV v2u ldsc2(rsrc_t rs, int off) { return __builtin_amdgcn_raw_buffer_load_b64(rs, off, 0, 16); }
DEV void stsc(rsrc_t rs, int off, v4u v) { __builtin_amdgcn_raw_buffer_store_b128(v, rs, off, 0, 16); }
DEV void stsc2(rsrc_t rs, int off, v2u v) { __builtin_amdgcn_raw_buffer_store_b64(v, rs, off, 0, 16); }
DEV unsigned umax4(v4u v) { return max(max(v.x, v.y), max(v.z, v.w)); }
DEV v4u ldg(const void* p) { return __builtin_nontemporal_load((const GAS v4u*)p); }
template <int IMM> DEV void dmas(const void* base, unsigned voff, int ldst) {
    asm volatile("s_mov_b32 m0, %2\n\ts_nop 0\n\tglobal_load_lds_dwordx4 %0, %1 offset:%3 nt" :: "v"(voff), "s"(base), "s"(ldst), "i"(IMM) : "memory");
}
template <int N> DEV void wait_vm() { asm volatile("s_waitcnt vmcnt(%0)" :: "i"(N) : "memory"); }
DEV void wait_lgkm() { asm volatile("s_waitcnt lgkmcnt(0)" ::: "memory"); }
DEV v4u ldgo(const void* base, unsigned off) { return __builtin_nontemporal_load((const GAS v4u*)((const GAS char*)base + off)); }
DEV v2u trread(LAS unsigned char* p) { return __builtin_bit_cast(v2u, __builtin_amdgcn_ds_read_tr16_b64_v4i16((LAS s16x4*)p)); }
DEV v4u lds128(LAS unsigned char* p) { return *(LAS v4u*)p; }
constexpr int n_issued(int i) { return i < 0 ? 0 : ((i + (i < A_SLOWN ? A_W0 : RS)) < 16 ? (i + (i < A_SLOWN ? A_W0 : RS)) : 16); }
template <int V> struct IC { static constexpr int value = V; };
template <int B, int E, class F> DEV void static_for(F&& f) { if constexpr (B < E) { f(IC<B>{}); static_for<B + 1, E>(f); } }
#define COMPILER_FENCE() asm volatile("" ::: "memory")
#define WAIT_VM(n) __builtin_amdgcn_s_waitcnt(0x0F70 | ((n) & 15) | (((n) >> 4) << 14))
#define DRAIN() asm volatile("s_waitcnt vmcnt(0)" ::: "memory")
DEV void bar() { COMPILER_FENCE(); __builtin_amdgcn_s_waitcnt(0xC07F); __builtin_amdgcn_s_barrier(); COMPILER_FENCE(); }
#define PIN() __builtin_amdgcn_sched_barrier(0)

__global__ void __launch_bounds__(512, 2)
mla_decode_kernel(const u16* __restrict__ x, const u16* __restrict__ ckv, const u16* __restrict__ wq, const u16* __restrict__ wuk,
                  const u16* __restrict__ wuv, const u16* __restrict__ wo, const u16* __restrict__ w1, const u16* __restrict__ b1,
                  const u16* __restrict__ w2, const u16* __restrict__ b2, u16* __restrict__ out, unsigned char* ws) {
    extern __shared__ __attribute__((aligned(16))) unsigned char smem[];
    LAS unsigned char* L = (LAS unsigned char*)smem;
    u64 t_first; asm volatile("s_memrealtime %0" : "=s"(t_first));
    __builtin_amdgcn_sched_barrier(0);
    const int tid = threadIdx.x, lane = tid & 63;
    {
        u64 pc;
        const unsigned line = tid >> 1;
        asm volatile("s_getpc_b64 %0" : "=s"(pc));
        asm volatile("s_mov_b32 m0, %2\n\ts_nop 0\n\tglobal_load_lds_dword %0, %1" :: "v"((line < CODE_LINES ? line : 0u) * 128u), "s"(pc), "i"(LDS_BYTES - 256) : "memory");
    }
    const u64 t_start = __builtin_amdgcn_s_memrealtime();
    asm volatile("" :: "s"(x), "s"(ckv), "s"(wq), "s"(wuk), "s"(wuv), "s"(wo), "s"(w1), "s"(b1), "s"(w2), "s"(b2), "s"(out), "s"(ws));
    u64 epoch = *(const __attribute__((address_space(4))) u64*)(ws + WS_E);
    const int w = __builtin_amdgcn_readfirstlane(tid >> 6);
    const int b = blockIdx.x, xc = b & 7, ii = b >> 3;
    const int n16 = lane & 15, g4 = lane >> 4;
    const rsrc_t rs = __builtin_amdgcn_make_buffer_rsrc(ws, 0, WS_END, 0x00020000);
    const v4u SV = {SENT, SENT, SENT, SENT};
    const float L2E = 1.4426950408889634f;
    u64 amp_t = t_start;
    if (AMPMODE >= 2 && tid == 0) __hip_atomic_store((u64*)(ws + (64u << 20)) + b, t_first, __ATOMIC_RELAXED, __HIP_MEMORY_SCOPE_AGENT);

    if (__builtin_expect(ii < 16, 1)) {
        const int hk = 2 * xc + (ii >> 3), e = ii & 7;
        const int hf = ii, ef = xc;
        const bool has_mlp = ii < 14;
        const int fb = 14 * xc + ii;
        const int wq7 = w < 7 ? w : 6;
        unsigned tag_lo, tag_hi;
        if (w < 4) {
            __builtin_amdgcn_s_setprio(3);
            const v4u xv = ldgo(x, (lane < 56 ? lane : 0) * 16);
            v4u qa[7];
            const unsigned oq = (224 * (ef & 1) + 56 * w + (lane >> 3)) * 8192 + hf * 512 + (ef >> 1) * 128 + (lane & 7) * 16;
#pragma unroll
            for (int u = 0; u < 7; ++u) qa[u] = ldgo(wq, oq + u * 8 * 8192);
            PIN();
            __builtin_amdgcn_s_barrier();
            PIN();
            STAMP(12);
            float xf[8] = {bflo(xv.x), bfhi(xv.x), bflo(xv.y), bfhi(xv.y), bflo(xv.z), bfhi(xv.z), bflo(xv.w), bfhi(xv.w)};
            float s = ((xf[0] + xf[1]) + (xf[2] + xf[3])) + ((xf[4] + xf[5]) + (xf[6] + xf[7]));
            if (lane >= 56) s = 0.f;
            const float mean = wave_sum(s) * (1.f / 448.f);
            float s2 = 0.f;
#pragma unroll
            for (int j = 0; j < 8; ++j) { float d = xf[j] - mean; s2 += d * d; }
            if (lane >= 56) s2 = 0.f;
            const float var = wave_sum(s2) * (1.f / 448.f);
            const float mb = rbf(mean);
            const float den = rbf(__builtin_amdgcn_sqrtf(rbf(rbf(var) + 1e-5f)));
            const float rden = __builtin_amdgcn_rcpf(den);
            if (lane < 56) {
                f32x4 o0, o1;
#pragma unroll
                for (int j = 0; j < 4; ++j) { o0[j] = rbf(rbf(xf[j] - mb) * rden); o1[j] = rbf(rbf(xf[4 + j] - mb) * rden); }
                *(LAS f32x4*)(L + LW_XN + lane * 32) = o0;
                *(LAS f32x4*)(L + LW_XN + lane * 32 + 16) = o1;
            }
            COMPILER_FENCE();
            float xm[7];
#pragma unroll
            for (int u = 0; u < 7; ++u) xm[u] = *(LAS float*)(L + LW_XN + (224 * (ef & 1) + 56 * w + 8 * u + (lane >> 3)) * 4);
            asm volatile("" : "+s"(epoch));
            const u64 tag = epoch * 6364136223846793005ull + 1442695040888963407ull;
            tag_lo = (unsigned)tag; tag_hi = (unsigned)(tag >> 32);
            PIN();
            STAMP(13);
            float acc[8];
#pragma unroll
            for (int j = 0; j < 8; ++j) acc[j] = 0.f;
#pragma unroll
            for (int u = 0; u < 7; ++u) {
                acc[0] += xm[u] * bflo(qa[u].x); acc[1] += xm[u] * bfhi(qa[u].x); acc[2] += xm[u] * bflo(qa[u].y); acc[3] += xm[u] * bfhi(qa[u].y);
                acc[4] += xm[u] * bflo(qa[u].z); acc[5] += xm[u] * bfhi(qa[u].z); acc[6] += xm[u] * bflo(qa[u].w); acc[7] += xm[u] * bfhi(qa[u].w);
                PIN();
            }
            float r0 = add32(acc[0], acc[4]), r1 = add32(acc[1], acc[5]), r2 = add32(acc[2], acc[6]), r3 = add32(acc[3], acc[7]);
            float s0 = add16(r0, r2), s1 = add16(r1, r3);
            s0 += dpp<DPP_ROR8>(s0); s1 += dpp<DPP_ROR8>(s1);
            if ((lane & 8) == 0) {
                f32x2 o = {s0, s1};
                *(LAS f32x2*)(L + LW_QP + w * 256 + (8 * (lane & 7) + 4 * (lane >> 5) + 2 * ((lane >> 4) & 1)) * 4) = o;
            }
            STAMP(1);
            wait_lgkm();
            if (lane == 0) __hip_atomic_fetch_add((LAS unsigned*)(L + LW_CNT), 1u, __ATOMIC_RELAXED, __HIP_MEMORY_SCOPE_WORKGROUP);
            const unsigned th = (tag_lo >> 8) | (tag_hi << 24);
            if (w == 0) {
                for (int spin = 0; spin < (1 << 20) && *(volatile LAS unsigned*)(L + LW_CNT) < 4u; ++spin) {}
                COMPILER_FENCE();
                f32x2 a = {0.f, 0.f};
#pragma unroll
                for (int v = 0; v < 4; ++v) a += *(LAS f32x2*)(L + LW_QP + v * 256 + (lane & 31) * 8);
                const float ax = a.x, ay = a.y;
                v4u gr = {(f2u(ax) >> 8) | (tag_lo << 24), th, (f2u(ay) >> 8) | (tag_lo << 24), th};
                stsc(rs, lane < 32 ? WS_Q1 + hf * 4096 + ef * 512 + lane * 16 : OOB, gr);
                STAMP(2);
            }
            __builtin_amdgcn_s_sleep(H1_SLEEP);
            {
                const int off = WS_Q1 + hf * 4096 + w * 1024 + lane * 8;
                v2u v0, v1;
                v2u fl = ldsc2(rs, WS_FLAG + (w * 32 + (lane & 31)) * 8);
                for (int spin = 0;; ++spin) {
                    v0 = ldsc2(rs, off); v1 = ldsc2(rs, off + 512);
                    const bool ok = ((v0.x ^ (tag_lo << 24)) | (v1.x ^ (tag_lo << 24))) < 0x1000000u && v0.y == th && v1.y == th;
                    if (__all(ok) || spin > SPIN_CAP) break;
                    COMPILER_FENCE();
                }
                for (int spin = 0; !__all(fl.x == tag_lo && fl.y == tag_hi) && spin < SPIN_CAP; ++spin) {
                    __builtin_amdgcn_s_sleep(8);
                    fl = ldsc2(rs, WS_FLAG + (w * 32 + (lane & 31)) * 8);
                    COMPILER_FENCE();
                }
                const float qv = u2f(v0.x << 8) + u2f(v1.x << 8);
                *(LAS u16*)(L + LW_QV + (w * 64 + lane) * 2) = (u16)pk2(qv, qv);
            }
            STAMP(3);
            __builtin_amdgcn_s_setprio(0);
            bar();
        } else {
            const int kw = w - 4;
            if (kw == 0) { const v2u z = {0u, 0u}; *(LAS v2u*)(L + LW_Z + lane * 8) = z; *(LAS unsigned*)(L + LW_CNT) = 0u; }
            PIN();
            bar();
            v4u wk[16];
            const unsigned ok = (128 * ef + 32 * kw + (n16 >> 2)) * 8192 + hf * 512 + g4 * 64 + (n16 & 3) * 16;
#pragma unroll
            for (int u = 0; u < 16; ++u) wk[u] = ldgo(wuk, ok + (u >> 1) * 4 * 8192 + (u & 1) * 256);
            PIN();
            asm volatile("" : "+s"(epoch));
            const u64 tag = epoch * 6364136223846793005ull + 1442695040888963407ull;
            tag_lo = (unsigned)tag; tag_hi = (unsigned)(tag >> 32);
            STAMP(1);
            bar();
            v4u qf[8];
#pragma unroll
            for (int u = 0; u < 8; ++u) qf[u] = lds128(L + ((n16 >> 2) == (u >> 1) ? LW_QV : LW_Z) + (u & 1) * 256 + g4 * 64 + (n16 & 3) * 16);
            PIN();
            f32x4 d0 = {0.f, 0.f, 0.f, 0.f}, d1 = {0.f, 0.f, 0.f, 0.f};
#pragma unroll
            for (int u = 0; u < 8; ++u) d0 = mfma(wk[u], qf[u], d0);
#pragma unroll
            for (int u = 0; u < 8; ++u) d1 = mfma(wk[8 + u], qf[u], d1);
            float v0 = (lane & 2) ? ((lane & 1) ? d0.w : d0.z) : ((lane & 1) ? d0.y : d0.x);
            float v1 = (lane & 2) ? ((lane & 1) ? d1.w : d1.z) : ((lane & 1) ? d1.y : d1.x);
            v0 += dpp<DPP_X1>(v0); v1 += dpp<DPP_X1>(v1); v0 += dpp<DPP_X2>(v0); v1 += dpp<DPP_X2>(v1);
            const float v = ((lane & 1) ? v1 : v0) * 0.0625f;
            unsigned vb = pk2(v, v) & 0xffffu;
            vb = vb < 0xfffeu ? vb : 0xfffeu;
            if ((lane & 2) == 0) *(LAS u16*)(L + LW_QA + (32 * kw + 16 * (lane & 1) + (n16 & 12) + g4) * 2) = (u16)vb;
            STAMP(3);
        }
        const u64 tag = ((u64)tag_hi << 32) | tag_lo;
        bar();
        if (w == 4) stsc(rs, lane < 16 ? WS_Q2 + hf * 2048 + ef * 256 + lane * 16 : OOB, lds128(L + LW_QA + n16 * 16));
        PIN();
        STAMP(4);
        {
            const unsigned vo = (512 * hk + 64 * w + g4) * 2048 + e * 256;
#pragma unroll
            for (int u = 0; u < 16; ++u) {
                if (u >= COLS_WIN) wait_vm<COLS_WIN - 1>();
                dmas<0>(ckv, vo + u * 8192 + ((n16 ^ ((g4 << 2) | (u & 3))) * 16), LW_COLS + (64 * w + 4 * u) * 256);
            }
        }
        PIN();
        STAMP(5);
        wait_vm<0>();
        bar();
        STAMP(6);
        v4u vf[16];
        {
            const int q = n16 >> 2, p = lane & 3;
#pragma unroll
            for (int t = 0; t < 2; ++t) {
                LAS unsigned char* base = L + LW_COLS + (8 * g4 + 4 * t + q) * 256 + (((2 * w + (p >> 1)) ^ ((q << 2) | ((2 * g4 + t) & 3))) * 16) + 8 * (p & 1);
#pragma unroll
                for (int ks = 0; ks < 16; ++ks) {
                    v2u r = trread(base + ks * 8192);
                    if (t == 0) { vf[ks].x = r.x; vf[ks].y = r.y; } else { vf[ks].z = r.x; vf[ks].w = r.y; }
                }
            }
        }
        PIN();
        const int off3 = WS_S3 + (hk * 32 + (lane >> 2)) * 512 + w * 64 + (lane & 3) * 16;
        if (w == 0) {
            for (int spin = 0; spin < SPIN_CAP; ++spin) {
                const unsigned hv = __builtin_amdgcn_raw_buffer_load_b32(rs, WS_H3 + (hk * 8 + (lane & 7)) * 128, 0, 16);
                if (__all(hv != SENT)) break;
                COMPILER_FENCE();
            }
            if (H3_SLEEP) __builtin_amdgcn_s_sleep(H3_SLEEP);
        }
        STAMP(14);
        bar();
        v4u sc0 = ldsc(rs, off3), sc1 = ldsc(rs, off3 + 16 * 512);
        for (int spin = 0; spin < SPIN_CAP; ++spin) {
            const bool ok0 = __all(umax4(sc0) != SENT), ok1 = __all(umax4(sc1) != SENT);
            if (ok0 && ok1) break;
            if (!ok0) sc0 = ldsc(rs, off3);
            if (!ok1) sc1 = ldsc(rs, off3 + 16 * 512);
            COMPILER_FENCE();
        }
        STAMP(7);
        float sm_m, sm_l;
        {
            float s[16] = {bflo(sc0.x), bfhi(sc0.x), bflo(sc0.y), bfhi(sc0.y), bflo(sc0.z), bfhi(sc0.z), bflo(sc0.w), bfhi(sc0.w),
                           bflo(sc1.x), bfhi(sc1.x), bflo(sc1.y), bfhi(sc1.y), bflo(sc1.z), bfhi(sc1.z), bflo(sc1.w), bfhi(sc1.w)};
            float m = s[0];
#pragma unroll
            for (int j = 1; j < 16; ++j) m = fmaxf(m, s[j]);
            m = fmaxf(m, dpp<DPP_X1>(m)); m = fmaxf(m, dpp<DPP_ROR4>(m)); m = fmaxf(m, dpp<DPP_ROR8>(m));
            m = max16(m); m = max32(m);
            const float mm = -m * L2E;
#pragma unroll
            for (int j = 0; j < 16; ++j) s[j] = __builtin_amdgcn_exp2f(__builtin_fmaf(s[j], L2E, mm));
            v4u p0 = {pk2(s[0], s[1]), pk2(s[2], s[3]), pk2(s[4], s[5]), pk2(s[6], s[7])};
            v4u p1 = {pk2(s[8], s[9]), pk2(s[10], s[11]), pk2(s[12], s[13]), pk2(s[14], s[15])};
            const int tile = lane >> 2, head = 2 * w + ((lane >> 1) & 1), gp = 2 * (tile & 1) + (lane & 1);
            LAS unsigned char* dst = L + LW_PF + (tile >> 1) * 1024 + head * 64 + ((gp ^ ((-(head >> 2)) & 3)) * 16);
            *(LAS v4u*)dst = p0;
            *(LAS v4u*)(dst + 8192) = p1;
            PIN();
            sm_m = m;
            sm_l = (((s[0] + s[1]) + (s[2] + s[3])) + ((s[4] + s[5]) + (s[6] + s[7]))) + (((s[8] + s[9]) + (s[10] + s[11])) + ((s[12] + s[13]) + (s[14] + s[15])));
        }
        bar();
        v4u m1[2], m2[2];
        unsigned b1v = 0;
        {
            v4u pf[16];
            LAS unsigned char* pb = L + LW_PF + n16 * 64 + ((g4 ^ ((-(n16 >> 2)) & 3)) * 16);
#pragma unroll
            for (int ks = 0; ks < 16; ++ks) pf[ks] = lds128(pb + ks * 1024);
            PIN();
            f32x4 d = {0.f, 0.f, 0.f, 0.f};
#pragma unroll
            for (int ks = 0; ks < 16; ++ks) d = mfma(vf[ks], pf[ks], d);
            float l = sm_l;
            l += dpp<DPP_X1>(l); l += dpp<DPP_ROR4>(l); l += dpp<DPP_ROR8>(l); l = add16(l, l); l = add32(l, l);
            if ((lane & 61) == 0) { v2u o = {nosent(f2u(sm_m)), nosent(f2u(l))}; *(LAS v2u*)(L + LW_ST + (2 * w + (lane >> 1)) * 8) = o; }
            v2u o = {nosent(pk2(d.x, d.y)), nosent(pk2(d.z, d.w))};
            *(LAS v2u*)(L + LW_TR + n16 * 272 + w * 32 + g4 * 8) = o;
        }
        bar();
        if (w < 4) stsc(rs, WS_P4 + ((4 * w + g4) * 16 + hk) * 2048 + e * 256 + n16 * 16, lds128(L + LW_TR + (4 * w + g4) * 272 + n16 * 16));
        else if (w == 4 && e == 0) { if (lane < 8) stsc(rs, WS_ST4 + hk * 128 + lane * 16, lds128(L + LW_ST + lane * 16)); }
        PIN();
        STAMP(8);
        if (has_mlp) {
            {
                const char* p1 = (const char*)w1 + (size_t)(64 * wq7 + (lane >> 1)) * 3584 + fb * 32 + (lane & 1) * 16;
                m1[0] = ldg(p1); m1[1] = ldg(p1 + 32 * 3584);
                const char* p2 = (const char*)w2 + (size_t)(fb * 16 + 2 * w) * 896 + (lane < 56 ? lane : 0) * 16;
                m2[0] = ldg(p2); m2[1] = ldg(p2 + 896);
                b1v = *(const GAS unsigned*)((const char*)b1 + fb * 32 + w * 4);
            }
            PIN();
            if (w < 7) {
                float acc[8];
#pragma unroll
                for (int j = 0; j < 8; ++j) acc[j] = 0.f;
#pragma unroll
                for (int u = 0; u < 2; ++u) {
                    const float xm = *(LAS float*)(L + LW_XN + (64 * w + 32 * u + (lane >> 1)) * 4);
                    acc[0] += xm * bflo(m1[u].x); acc[1] += xm * bfhi(m1[u].x); acc[2] += xm * bflo(m1[u].y); acc[3] += xm * bfhi(m1[u].y);
                    acc[4] += xm * bflo(m1[u].z); acc[5] += xm * bfhi(m1[u].z); acc[6] += xm * bflo(m1[u].w); acc[7] += xm * bfhi(m1[u].w);
                }
                float r0 = add32(acc[0], acc[4]), r1 = add32(acc[1], acc[5]), r2 = add32(acc[2], acc[6]), r3 = add32(acc[3], acc[7]);
                float s0 = add16(r0, r2), s1 = add16(r1, r3);
                s0 += dpp<DPP_X2>(s0); s1 += dpp<DPP_X2>(s1);
                s0 += dpp<DPP_ROR4>(s0); s1 += dpp<DPP_ROR4>(s1); s0 += dpp<DPP_ROR8>(s0); s1 += dpp<DPP_ROR8>(s1);
                if ((lane & 14) == 0) {
                    f32x2 o = {s0, s1};
                    *(LAS f32x2*)(L + LW_HP + w * 64 + (8 * (lane & 1) + 4 * (lane >> 5) + 2 * ((lane >> 4) & 1)) * 4) = o;
                }
            }
            bar();
            f32x2 hh = {0.f, 0.f};
#pragma unroll
            for (int v = 0; v < 7; ++v) hh += *(LAS f32x2*)(L + LW_HP + v * 64 + w * 8);
            float h0 = rbf(rbf(hh.x) + bflo(b1v)), h1 = rbf(rbf(hh.y) + bfhi(b1v));
            h0 = h0 > 0.f ? h0 : 0.f; h1 = h1 > 0.f ? h1 : 0.f;
            h0 = rbf(h0 * h0); h1 = rbf(h1 * h1);
            if (lane < 56) {
                f32x4 o0 = {h0 * bflo(m2[0].x) + h1 * bflo(m2[1].x), h0 * bfhi(m2[0].x) + h1 * bfhi(m2[1].x),
                            h0 * bflo(m2[0].y) + h1 * bflo(m2[1].y), h0 * bfhi(m2[0].y) + h1 * bfhi(m2[1].y)};
                f32x4 o1 = {h0 * bflo(m2[0].z) + h1 * bflo(m2[1].z), h0 * bfhi(m2[0].z) + h1 * bfhi(m2[1].z),
                            h0 * bflo(m2[0].w) + h1 * bflo(m2[1].w), h0 * bfhi(m2[0].w) + h1 * bfhi(m2[1].w)};
                *(LAS f32x4*)(L + LW_COLS + w * 1792 + lane * 32) = o0;
                *(LAS f32x4*)(L + LW_COLS + w * 1792 + lane * 32 + 16) = o1;
            }
            bar();
            if (tid < 112) {
                f32x4 a = {0.f, 0.f, 0.f, 0.f};
#pragma unroll
                for (int v = 0; v < 8; ++v) a += *(LAS f32x4*)(L + LW_COLS + v * 1792 + tid * 16);
                float a0 = a.x, a1 = a.y, a2 = a.z, a3 = a.w;
                v4u o = {nosent(f2u(a0)), nosent(f2u(a1)), nosent(f2u(a2)), nosent(f2u(a3))};
                stsc(rs, WS_M5 + (tid >> 3) * (112 * 128) + fb * 128 + (tid & 7) * 16, o);
            }
            STAMP(9);
        } else {
            const int fi = 2 * xc + (ii - 14);
            if (fi < 14) {
                const unsigned xb = *(const GAS u16*)(x + fi * 32 + (lane & 31)), b2b = *(const GAS u16*)(b2 + fi * 32 + (lane & 31));
                const int offm = WS_M5 + fi * (112 * 128) + wq7 * 2048 + lane * 16, offs = WS_P5 + fi * 16384 + w * 2048 + lane * 16;
                v4u a0 = ldsc(rs, offm), a1 = ldsc(rs, offm + 1024), c0 = ldsc(rs, offs), c1 = ldsc(rs, offs + 1024);
                for (int spin = 0; spin < SPIN_CAP; ++spin) {
                    const bool k0 = __all(umax4(a0) != SENT), k1 = __all(umax4(a1) != SENT), k2 = __all(umax4(c0) != SENT), k3 = __all(umax4(c1) != SENT);
                    if (k0 && k1 && k2 && k3) break;
                    if (!k0) a0 = ldsc(rs, offm);
                    if (!k1) a1 = ldsc(rs, offm + 1024);
                    if (!k2) c0 = ldsc(rs, offs);
                    if (!k3) c1 = ldsc(rs, offs + 1024);
                    COMPILER_FENCE();
                }
                STAMP(10);
                {
                    const f32x4 am = __builtin_bit_cast(f32x4, a0) + __builtin_bit_cast(f32x4, a1);
                    const f32x4 as = __builtin_bit_cast(f32x4, c0) + __builtin_bit_cast(f32x4, c1);
                    float s0 = add16(add32(am.x, am.z), add32(am.y, am.w)), s1 = add16(add32(as.x, as.z), add32(as.y, as.w));
                    s0 += dpp<DPP_ROR8>(s0); s1 += dpp<DPP_ROR8>(s1);
                    f32x2 o = {s0, s1};
                    if ((lane & 8) == 0) *(LAS f32x2*)(L + LW_FR + w * 256 + ((lane & 7) * 4 + 2 * (lane >> 5) + ((lane >> 4) & 1)) * 8) = o;
                }
                bar();
                if (tid < 32) {
                    f32x2 a = {0.f, 0.f};
#pragma unroll
                    for (int v = 0; v < 7; ++v) a += *(LAS f32x2*)(L + LW_FR + v * 256 + tid * 8);
                    const float sv = a.y + *(LAS float*)(L + LW_FR + 7 * 256 + tid * 8 + 4);
                    const float o2 = rbf(rbf(a.x) + bflo(b2b));
                    const float gate = rbf(o2 * __builtin_amdgcn_rcpf(1.0f + __builtin_amdgcn_exp2f(-o2 * L2E)));
                    const float o = bflo(xb) + rbf(rbf(sv) * gate);
                    *(GAS u16*)(out + fi * 32 + tid) = (u16)pk2(o, o);
                    if (fi == 0 && tid == 0) *(GAS u64*)(ws + WS_E) = tag;
                }
                STAMP(11);
            }
        }
    } else {
        const int ia = ii - 16, ai = xc * 16 + ia;
        const int hd = 2 * xc + (ia >> 3), ch = (ia >> 2) & 1, dq = ia & 3;
        if (w < 4) {
            v4u a[32], q[16];
            const int r8 = lane >> 3, pos = lane & 7;
            const unsigned vo0 = (64 * ai + 16 * w + r8) * 2048 + ((pos ^ (r8 >> 1)) * 16);
            const unsigned vo1 = (64 * ai + 16 * w + 8 + r8) * 2048 + ((pos ^ (4 + (r8 >> 1))) * 16);
            const int ring = LA_RING + w * RS * 2048;
            LAS unsigned char* ra0 = L + ring + n16 * 128 + ((g4 ^ (n16 >> 1)) * 16);
            LAS unsigned char* ra1 = L + ring + n16 * 128 + (((4 + g4) ^ (n16 >> 1)) * 16);
            LAS unsigned char* qb = L + LA_Q + n16 * 64 + ((g4 ^ ((-(n16 >> 2)) & 3)) * 16);
            f32x4 d = {0.f, 0.f, 0.f, 0.f};
            static_for<0, 16>([&](auto I) {
                constexpr int i = I.value;
                static_for<n_issued(i - 1), n_issued(i)>([&](auto J) {
                    constexpr int j = J.value;
                    dmas<j * 128>(ckv, vo0, ring + (j % RS) * 2048 - j * 128);
                    dmas<j * 128>(ckv, vo1, ring + (j % RS) * 2048 + 1024 - j * 128);
                });
                if constexpr (n_issued(i) == 16 && n_issued(i - 1) < 16) {
                    STAMP(1);
                    bar();
                    STAMP(2);
#pragma unroll
                    for (int j = 0; j < 16; ++j) q[j] = lds128(qb + j * 1024);
                    PIN();
#pragma unroll
                    for (int j = 0; j < 16; ++j) d = mfma(a[j], q[j], d);
                    PIN();
#pragma unroll
                    for (int j = 0; j < 16; ++j) q[j] = lds128(qb + (16 + j) * 1024);
                    PIN();
#pragma unroll
                    for (int j = 16; j < 2 * i; ++j) d = mfma(a[j], q[j - 16], d);
                    PIN();
                }
                wait_vm<2 * (n_issued(i) - i - 1)>();
                a[2 * i] = lds128(ra0 + (i % RS) * 2048);
                a[2 * i + 1] = lds128(ra1 + (i % RS) * 2048);
                if constexpr (n_issued(i) < 16) {
                    asm volatile("s_waitcnt lgkmcnt(0)" : "+v"(a[2 * i]), "+v"(a[2 * i + 1]) :: "memory");
                } else {
                    d = mfma(a[2 * i], q[2 * i - 16], d);
                    d = mfma(a[2 * i + 1], q[2 * i + 1 - 16], d);
                    PIN();
                }
            });
            unsigned a0 = nosent(pk2(d.x, d.y)), a1 = nosent(pk2(d.z, d.w)), c0 = a0, c1 = a1;
            swap16(a0, c0); swap16(a1, c1);
            v4u o = {a0, a1, c0, c1};
            stsc(rs, (g4 & 1) == 0 ? WS_S3 + (ai * 4 + w) * 512 + n16 * 32 + (g4 >> 1) * 16 : OOB, o);
            PIN();
            STAMP(3);
        } else {
            const int tw = w - 4;
            if (tw == 0) {
#pragma unroll
                for (int u = 0; u < 10; ++u) stsc(rs, WS_Q2 + ai * 10240 + u * 1024 + lane * 16, SV);
            }
            PIN();
            asm volatile("" : "+s"(epoch));
            const u64 tag = epoch * 6364136223846793005ull + 1442695040888963407ull;
            const unsigned tag_lo = (unsigned)tag, tag_hi = (unsigned)(tag >> 32);
            if (tw == 0) {
                wait_vm<0>();
                v2u t = {tag_lo, tag_hi};
                stsc2(rs, lane == 0 ? WS_FLAG + ai * 8 : OOB, t);
            }
            STAMP(1);
            while (__builtin_amdgcn_s_memrealtime() - t_start < FLAG_TICKS) __builtin_amdgcn_s_sleep(2);
            for (int spin = 0;; ++spin) {
                v4u v = ldsc(rs, WS_FLAG + lane * 16);
                const bool ok = v.x == tag_lo && v.y == tag_hi && v.z == tag_lo && v.w == tag_hi;
                if (__all(ok) || spin > SPIN_CAP) break;
                __builtin_amdgcn_s_sleep(8);
                COMPILER_FENCE();
            }
            STAMP(2);
            while (__builtin_amdgcn_s_memrealtime() - t_start < H2_TICKS) __builtin_amdgcn_s_sleep(1);
            for (int spin = 0; spin < SPIN_CAP; ++spin) {
                v4u v = ldsc(rs, WS_Q2 + tw * 8192 + (ia & 7) * 1024 + lane * 16);
                if (__all(umax4(v) != SENT)) break;
                COMPILER_FENCE();
            }
            STAMP(12);
            {
                v4u qv[8];
                const int off = WS_Q2 + tw * 8192 + lane * 16;
#pragma unroll
                for (int u = 0; u < 8; ++u) qv[u] = ldsc(rs, off + u * 1024);
                for (int spin = 0; spin < SPIN_CAP; ++spin) {
                    bool all = true;
#pragma unroll
                    for (int u = 0; u < 8; ++u) {
                        if (!__all(umax4(qv[u]) != SENT)) { all = false; qv[u] = ldsc(rs, off + u * 1024); }
                    }
                    if (all) break;
                    COMPILER_FENCE();
                }
                LAS unsigned char* dst = L + LA_Q + (lane >> 2) * 1024 + tw * 256 + (((lane & 3) ^ ((-tw) & 3)) * 16);
#pragma unroll
                for (int u = 0; u < 8; ++u) *(LAS v4u*)(dst + (u & 1) * 16384 + (u >> 1) * 64) = qv[u];
            }
            STAMP(3);
            bar();
            if (tw == 0) __builtin_amdgcn_raw_buffer_store_b32(0u, rs, lane == 0 ? WS_H3 + ai * 128 : OOB, 0, 16);
            {
                const int r8 = lane >> 3, pos = lane & 7;
                const unsigned vo = (512 * ch + 128 * tw + r8) * 8192 + hd * 512 + dq * 128;
#pragma unroll
                for (int u = 0; u < TAIL_X; ++u)
                    dmas<0>(wuv, vo + u * 65536 + ((pos ^ ((((r8 >> 1) & 1) | ((u & 1) << 1)) << 1)) * 16), LA_UV + (128 * tw + 8 * u) * 128);
            }
            STAMP(4);
        }
        bar();
        {
            const int r8 = lane >> 3, pos = lane & 7, tq = w & 3, th = w >> 2;
            const unsigned vo = (512 * ch + 128 * tq + r8) * 8192 + hd * 512 + dq * 128;
#pragma unroll
            for (int v = 0; v < (16 - TAIL_X) / 2; ++v) {
                const int u = TAIL_X + 2 * v + th;
                dmas<0>(wuv, vo + u * 65536 + ((pos ^ ((((r8 >> 1) & 1) | ((u & 1) << 1)) << 1)) * 16), LA_UV + (128 * tq + 8 * u) * 128);
            }
            const unsigned so = (hd * 256 + dq * 64 + 8 * w) * 896 + lane * 16;
            if (lane < 56) {
#pragma unroll
                for (int u = 0; u < 8; ++u) dmas<0>(wo, so + u * 896, LA_WO + (8 * w + u) * 928 + w * 128);
            }
        }
        PIN();
        wait_vm<0>();
        STAMP(5);
        bar();
        const int off4 = WS_P4 + (hd * 16 + (lane >> 3)) * 2048 + ch * 1024 + w * 128 + (lane & 7) * 16;
        const int soff = WS_ST4 + g4 * 512 + hd * 8;
        v2u s0 = ldsc2(rs, soff), s1 = ldsc2(rs, soff + 128), s2 = ldsc2(rs, soff + 256), s3 = ldsc2(rs, soff + 384);
        v4u c0 = ldsc(rs, off4), c1 = ldsc(rs, off4 + 8 * 2048);
        PIN();
        v4u uf[8], of[8];
        {
            const int q = n16 >> 2, p = lane & 3;
#pragma unroll
            for (int t = 0; t < 2; ++t) {
                const int sw = ((q >> 1) | ((g4 & 1) << 1)) << 1;
                LAS unsigned char* ub = L + LA_UV + (64 * w + 8 * g4 + 4 * t + q) * 128 + 8 * (p & 1);
                const int wq7 = w < 7 ? w : 6;
                LAS unsigned char* ob = L + LA_WO + (8 * g4 + 4 * t + q) * 928 + g4 * 128 + wq7 * 128 + p * 8;
#pragma unroll
                for (int ks = 0; ks < 2; ++ks)
#pragma unroll
                    for (int ct = 0; ct < 4; ++ct) {
                        v2u r = trread(ub + ks * 4096 + (((2 * ct + (p >> 1)) ^ sw) * 16));
                        v2u o = trread(ob + ks * (32 * 928 + 512) + ct * 32);
                        if (t == 0) { uf[ks * 4 + ct].x = r.x; uf[ks * 4 + ct].y = r.y; of[ks * 4 + ct].x = o.x; of[ks * 4 + ct].y = o.y; }
                        else { uf[ks * 4 + ct].z = r.x; uf[ks * 4 + ct].w = r.y; of[ks * 4 + ct].z = o.x; of[ks * 4 + ct].w = o.y; }
                    }
            }
        }
        PIN();
        STAMP(6);
        {
            for (int spin = 0; spin < SPIN_CAP; ++spin) {
                const bool ks = __all(max(max(max(s0.x, s0.y), max(s1.x, s1.y)), max(max(s2.x, s2.y), max(s3.x, s3.y))) != SENT);
                const bool kc = __all(max(umax4(c0), umax4(c1)) != SENT);
                if (ks && kc) break;
                if (!ks) { s0 = ldsc2(rs, soff); s1 = ldsc2(rs, soff + 128); s2 = ldsc2(rs, soff + 256); s3 = ldsc2(rs, soff + 384); }
                if (!kc) { c0 = ldsc(rs, off4); c1 = ldsc(rs, off4 + 8 * 2048); }
                COMPILER_FENCE();
            }
            STAMP(7);
            {
                LAS unsigned char* bn = L + LA_BN + w * 2304;
                *(LAS v4u*)(bn + (lane >> 3) * 144 + (lane & 7) * 16) = c0;
                *(LAS v4u*)(bn + (8 + (lane >> 3)) * 144 + (lane & 7) * 16) = c1;
                COMPILER_FENCE();
                c0 = lds128(bn + n16 * 144 + g4 * 16);
                c1 = lds128(bn + n16 * 144 + 64 + g4 * 16);
            }
            f32x4 d[4];
#pragma unroll
            for (int ct = 0; ct < 4; ++ct) {
                f32x4 z = {0.f, 0.f, 0.f, 0.f};
                d[ct] = mfma(c0, uf[ct], z);
            }
#pragma unroll
            for (int ct = 0; ct < 4; ++ct) d[ct] = mfma(c1, uf[4 + ct], d[ct]);
            const float m0 = u2f(s0.x), m1 = u2f(s1.x), m2 = u2f(s2.x), m3 = u2f(s3.x);
            float M = fmaxf(fmaxf(m0, m1), fmaxf(m2, m3));
            M = max16(M); M = max32(M);
            float f0 = __builtin_amdgcn_exp2f((m0 - M) * L2E), f1 = __builtin_amdgcn_exp2f((m1 - M) * L2E);
            float f2 = __builtin_amdgcn_exp2f((m2 - M) * L2E), f3 = __builtin_amdgcn_exp2f((m3 - M) * L2E);
            float Ls = (f0 * u2f(s0.y) + f1 * u2f(s1.y)) + (f2 * u2f(s2.y) + f3 * u2f(s3.y));
            Ls = add16(Ls, Ls); Ls = add32(Ls, Ls);
            const float rl = __builtin_amdgcn_rcpf(Ls);
            f0 *= rl; f1 *= rl; f2 *= rl; f3 *= rl;
            float cx[4];
#pragma unroll
            for (int ct = 0; ct < 4; ++ct) cx[ct] = (f0 * d[ct].x + f1 * d[ct].y) + (f2 * d[ct].z + f3 * d[ct].w);
            const float r0 = add32(cx[0], cx[2]), r1 = add32(cx[1], cx[3]);
            const float cv = add16(r0, r1);
            const float hi = rbf(cv), lo = cv - hi;
            const unsigned pk = pk2(hi, lo);
            *(LAS u16*)(L + LA_CT + w * 144 + lane * 2) = (u16)pk;
            *(LAS u16*)(L + LA_CT + (8 + w) * 144 + lane * 2) = (u16)(pk >> 16);
        }
        bar();
        if (w < 7) {
            const v4u a0 = lds128(L + LA_CT + n16 * 144 + g4 * 16), a1 = lds128(L + LA_CT + n16 * 144 + 64 + g4 * 16);
            f32x4 d[4];
#pragma unroll
            for (int ct = 0; ct < 4; ++ct) {
                f32x4 z = {0.f, 0.f, 0.f, 0.f};
                d[ct] = mfma(a0, of[ct], z);
            }
#pragma unroll
            for (int ct = 0; ct < 4; ++ct) d[ct] = mfma(a1, of[4 + ct], d[ct]);
            float sx[4];
#pragma unroll
            for (int ct = 0; ct < 4; ++ct) sx[ct] = (d[ct].x + d[ct].y) + (d[ct].z + d[ct].w);
            const float r0 = add32(sx[0], sx[2]), r1 = add32(sx[1], sx[3]);
            const float sv = add16(r0, r1);
            const unsigned su = nosent(f2u(sv));
            v4u o;
            o.x = su;
            o.y = (unsigned)__builtin_amdgcn_update_dpp(0, (int)su, 0x55, 0xf, 0xf, true);
            o.z = (unsigned)__builtin_amdgcn_update_dpp(0, (int)su, 0xAA, 0xf, 0xf, true);
            o.w = (unsigned)__builtin_amdgcn_update_dpp(0, (int)su, 0xFF, 0xf, 0xf, true);
            stsc(rs, (lane & 3) == 0 ? WS_P5 + (2 * w + (lane >> 5)) * 16384 + ai * 128 + (lane & 31) * 4 : OOB, o);
        }
        STAMP(8);
    }
    STAMP_OUT();
}

extern "C" void kernel_launch(void* const* d_in, const int* in_sizes, int n_in, void* d_out, int out_size, void* d_ws, size_t ws_size,
                              hipStream_t stream) {
    (void)hipFuncSetAttribute((const void*)mla_decode_kernel, hipFuncAttributeMaxDynamicSharedMemorySize, LDS_BYTES);
    mla_decode_kernel<<<256, 512, LDS_BYTES, stream>>>((const u16*)d_in[0], (const u16*)d_in[1], (const u16*)d_in[2], (const u16*)d_in[3],
                                                       (const u16*)d_in[4], (const u16*)d_in[5], (const u16*)d_in[7], (const u16*)d_in[8],
                                                       (const u16*)d_in[9], (const u16*)d_in[10], (u16*)d_out, (unsigned char*)d_ws);
}
```

```cpp
#include <hip/hip_runtime.h>
#include <hip/hip_bf16.h>

typedef unsigned short u16;
typedef unsigned long long u64;
typedef unsigned v4u __attribute__((ext_vector_type(4)));
typedef unsigned v2u __attribute__((ext_vector_type(2)));
typedef float f32x4 __attribute__((ext_vector_type(4)));
typedef float f32x2 __attribute__((ext_vector_type(2)));
typedef __bf16 bf16x8 __attribute__((ext_vector_type(8)));
typedef __bf16 bf16x2 __attribute__((ext_vector_type(2)));
typedef short s16x4 __attribute__((ext_vector_type(4)));
typedef int i32x4 __attribute__((ext_vector_type(4)));
typedef unsigned short u16x8 __attribute__((ext_vector_type(8)));
#define LAS __attribute__((address_space(3)))
#define GAS __attribute__((address_space(1)))
#define DEV __device__ __forceinline__
typedef __amdgpu_buffer_rsrc_t rsrc_t;

constexpr int WS_E = 0;
constexpr int WS_G1 = 8192;
constexpr int WS_Q1 = WS_G1 + 65536;
constexpr int WS_Q2 = WS_Q1 + 262144;
constexpr int WS_ST4 = WS_Q2 + 65536;
constexpr int WS_H3 = WS_ST4 + 8192;
constexpr int WS_H4 = WS_H3 + 16384;
constexpr int WS_P5 = WS_H4 + 32768;
constexpr int WS_M5 = WS_P5 + 28 * 16384;
constexpr int WS_P4 = WS_M5 + 28 * 112 * 128;
constexpr int WS_S3 = WS_P4 + 1048576;
constexpr int WS_END = WS_S3 + 2097152;
constexpr unsigned SENT = 0xffffffffu;
constexpr int SPIN_CAP = 1 << 14;
constexpr int OOB = 0x7ffffff0;
constexpr unsigned CODE_LINES = 116;

constexpr int LA_IMG = 0;
constexpr int LA_Q = 131072;
constexpr int LA_PF = LA_Q + 8192;
constexpr int LA_TR = LA_PF + 8192;
constexpr int LA_MX = LA_TR + 16 * 528;
constexpr int LA_XN = LA_PF;
constexpr int LA_QP = LA_XN + 2048;
constexpr int LA_GP = LA_QP + 7168;
constexpr int LW_UV = 0;
constexpr int LW_WO = 65536;
constexpr int LW_QV = LW_WO;
constexpr int LW_CT = LW_WO + 60416;
constexpr int LW_FS = LW_CT + 2304;
constexpr int LW_FR = LW_FS + 1024;
constexpr int L_Z = 159744;
constexpr int L_CNT = L_Z + 512;
constexpr int LDS_BYTES = 161280;
static_assert(LW_FR + 1024 <= L_Z && LA_MX + 1024 <= L_Z && LA_GP + 3584 <= LA_MX && L_Z + 576 <= LDS_BYTES - 256, "LDS map");

constexpr int A_WIN = 6;
constexpr int T_WIN = 6;
constexpr int T_SLOWN = 3;
constexpr int W_DELAY = 8;
constexpr int F_SLEEP = 12;

DEV unsigned f2u(float f) { return __builtin_bit_cast(unsigned, f); }
DEV float u2f(unsigned u) { return __builtin_bit_cast(float, u); }
DEV float bflo(unsigned w) { return u2f(w << 16); }
DEV float bfhi(unsigned w) { return u2f(w & 0xffff0000u); }
DEV unsigned pk2(float a, float b) { f32x2 p = {a, b}; bf16x2 r = __builtin_convertvector(p, bf16x2); return __builtin_bit_cast(unsigned, r); }
DEV float rbf(float a) { return bflo(pk2(a, a)); }
DEV unsigned nosent(unsigned u) { return u < 0xfffffffeu ? u : 0xfffffffeu; }
DEV f32x4 mfma(v4u a, v4u b, f32x4 c) {
    return __builtin_amdgcn_mfma_f32_16x16x32_bf16(__builtin_bit_cast(bf16x8, a), __builtin_bit_cast(bf16x8, b), c, 0, 0, 0);
}
template <int CTRL> DEV float dpp(float v) { return u2f((unsigned)__builtin_amdgcn_update_dpp(0, (int)f2u(v), CTRL, 0xf, 0xf, true)); }
constexpr int DPP_X1 = 0xB1, DPP_X2 = 0x4E, DPP_ROR4 = 0x124, DPP_ROR8 = 0x128;
DEV void swap16(unsigned& a, unsigned& b) { auto r = __builtin_amdgcn_permlane16_swap(a, b, false, false); a = r[0]; b = r[1]; }
DEV void swap32(unsigned& a, unsigned& b) { auto r = __builtin_amdgcn_permlane32_swap(a, b, false, false); a = r[0]; b = r[1]; }
DEV float add16(float a, float b) { unsigned x = f2u(a), y = f2u(b); swap16(x, y); return u2f(x) + u2f(y); }
DEV float add32(float a, float b) { unsigned x = f2u(a), y = f2u(b); swap32(x, y); return u2f(x) + u2f(y); }
DEV float fmx(float a, float b) { return __builtin_elementwise_maximum(a, b); }
DEV float max16(float a) { unsigned x = f2u(a), y = x; swap16(x, y); return fmx(u2f(x), u2f(y)); }
DEV float max32(float a) { unsigned x = f2u(a), y = x; swap32(x, y); return fmx(u2f(x), u2f(y)); }
DEV float wave_sum(float v) {
    v += dpp<DPP_X1>(v); v += dpp<DPP_X2>(v); v += dpp<DPP_ROR4>(v); v += dpp<DPP_ROR8>(v);
    v = add16(v, v); v = add32(v, v); return v;
}
DEV v4u ldsc(rsrc_t rs, int off) { return __builtin_amdgcn_raw_buffer_load_b128(rs, off, 0, 16); }
DEV v2u ldsc2(rsrc_t rs, int off) { return __builtin_amdgcn_raw_buffer_load_b64(rs, off, 0, 16); }
DEV void stsc(rsrc_t rs, int off, v4u v) { __builtin_amdgcn_raw_buffer_store_b128(v, rs, off, 0, 16); }
DEV void stsc2(rsrc_t rs, int off, v2u v) { __builtin_amdgcn_raw_buffer_store_b64(v, rs, off, 0, 16); }
DEV unsigned umax4(v4u v) { return max(max(v.x, v.y), max(v.z, v.w)); }
template <bool NT = true> DEV void dmas(const void* base, unsigned voff, int ldst) {
    if (NT) asm volatile("s_mov_b32 m0, %2\n\ts_nop 0\n\tglobal_load_lds_dwordx4 %0, %1 nt" :: "v"(voff), "s"(base), "s"(ldst) : "memory");
    else asm volatile("s_mov_b32 m0, %2\n\ts_nop 0\n\tglobal_load_lds_dwordx4 %0, %1" :: "v"(voff), "s"(base), "s"(ldst) : "memory");
}
template <int N> DEV void wait_vm() { asm volatile("s_waitcnt vmcnt(%0)" :: "i"(N) : "memory"); }
DEV void wait_lgkm() { asm volatile("s_waitcnt lgkmcnt(0)" ::: "memory"); }
DEV v4u ldgo(const void* base, unsigned off) {
    const GAS v4u* p = (const GAS v4u*)((const GAS char*)base + off);
    return __builtin_nontemporal_load(p);
}
DEV v2u trread(LAS unsigned char* p) { return __builtin_bit_cast(v2u, __builtin_amdgcn_ds_read_tr16_b64_v4i16((LAS s16x4*)p)); }
DEV v4u lds128(LAS unsigned char* p) { return *(LAS v4u*)p; }
template <int V> struct IC { static constexpr int value = V; };
template <int B, int E, class F> DEV void static_for(F&& f) { if constexpr (B < E) { f(IC<B>{}); static_for<B + 1, E>(f); } }
#define COMPILER_FENCE() asm volatile("" ::: "memory")
DEV void bar() { COMPILER_FENCE(); __builtin_amdgcn_s_waitcnt(0xC07F); __builtin_amdgcn_s_barrier(); COMPILER_FENCE(); }
#define PIN() __builtin_amdgcn_sched_barrier(0)

DEV void lds_post(LAS unsigned char* cnt, int lane) {
    wait_lgkm();
    if (lane == 0) __hip_atomic_fetch_add((LAS unsigned*)cnt, 1u, __ATOMIC_RELAXED, __HIP_MEMORY_SCOPE_WORKGROUP);
}
DEV void lds_wait(LAS unsigned char* cnt, unsigned target) {
#pragma nounroll
    for (int spin = 0; spin < (1 << 22) && *(volatile LAS unsigned*)cnt < target; ++spin) {}
    COMPILER_FENCE();
}
struct Tag { unsigned lo, hi, t24, th; };
DEV Tag make_tag(u64 tag) { Tag t; t.lo = (unsigned)tag; t.hi = (unsigned)(tag >> 32); t.t24 = t.lo << 24; t.th = (unsigned)(tag >> 8); return t; }
DEV void post_hint(rsrc_t rs, int off, Tag t) { v2u h = {t.lo, t.hi}; stsc2(rs, off, h); }
DEV void wait_hints(rsrc_t rs, int off, Tag t) {
#pragma nounroll
    for (int spin = 0; spin < SPIN_CAP; ++spin) {
        const v2u hv = ldsc2(rs, off);
        if (__all(((hv.x ^ t.lo) | (hv.y ^ t.hi)) == 0u)) break;
        COMPILER_FENCE();
    }
}
DEV unsigned tagdiff(v4u v, unsigned lo) { return (v.y ^ lo) | (v.w ^ lo); }
DEV void fma8(float (&acc)[8], float xm, v4u v) {
    acc[0] += xm * bflo(v.x); acc[1] += xm * bfhi(v.x); acc[2] += xm * bflo(v.y); acc[3] += xm * bfhi(v.y);
    acc[4] += xm * bflo(v.z); acc[5] += xm * bfhi(v.z); acc[6] += xm * bflo(v.w); acc[7] += xm * bfhi(v.w);
}

DEV v4u ld_hidden(const void* base, unsigned off) {
    v4u r;
    asm volatile("global_load_dwordx4 %0, %1, %2 nt" : "=v"(r) : "v"(off), "s"(base) : "memory");
    return r;
}
DEV void touch(const void* base, unsigned off) {
    asm volatile("s_mov_b32 m0, %2\n\ts_nop 0\n\tglobal_load_lds_dword %0, %1" :: "v"(off), "s"(base), "i"(LDS_BYTES - 256) : "memory");
}
DEV void touch_sc(const void* base, unsigned off) {
    asm volatile("s_mov_b32 m0, %2\n\ts_nop 0\n\tglobal_load_lds_dword %0, %1 sc1" :: "v"(off), "s"(base), "i"(LDS_BYTES - 256) : "memory");
}

__global__ void __launch_bounds__(512, 2)
mla_decode_kernel(const u16* __restrict__ x, const u16* __restrict__ ckv, const u16* __restrict__ wq, const u16* __restrict__ wuk,
                  const u16* __restrict__ wuv, const u16* __restrict__ wo, const u16* __restrict__ w1, const u16* __restrict__ b1,
                  const u16* __restrict__ w2, const u16* __restrict__ b2, u16* __restrict__ out, unsigned char* ws) {
    extern __shared__ __attribute__((aligned(16))) unsigned char smem[];
    LAS unsigned char* L = (LAS unsigned char*)smem;
    const int tid = threadIdx.x, lane = tid & 63;
    {
        u64 pc;
        asm volatile("s_getpc_b64 %0" : "=s"(pc));
        if (tid >= 512 - CODE_LINES)
            asm volatile("s_mov_b32 m0, %2\n\ts_nop 0\n\tglobal_load_lds_dword %0, %1" :: "v"((511u - tid) * 128u), "s"(pc), "i"(LDS_BYTES - 256) : "memory");
        if (tid >= 368) *(LAS unsigned*)(L + L_Z + (tid - 368) * 4) = 0u;
        PIN();
    }
    asm volatile("" :: "s"(x), "s"(ckv), "s"(wq), "s"(wuk), "s"(wuv), "s"(wo), "s"(w1), "s"(b1), "s"(w2), "s"(b2), "s"(out), "s"(ws));
    u64 epoch = *(const __attribute__((address_space(4))) u64*)(ws + WS_E);
    const int w = __builtin_amdgcn_readfirstlane(tid >> 6);
    const int b = blockIdx.x, xc = b & 7, ii = b >> 3;
    const int n16 = lane & 15, g4 = lane >> 4;
    const rsrc_t rs = __builtin_amdgcn_make_buffer_rsrc(ws, 0, WS_END, 0x00020000);
    const float L2E = 1.4426950408889634f;

    if (__builtin_expect(ii < 16, 1)) {
        const int ia = ii, ai = b;
        const int cq = ia & 3, kg = 4 * xc + (ia >> 2);
        const unsigned vo = (256 * kg + 32 * w + g4) * 2048 + cq * 512;
        auto tile_req = [&](auto U) {
            constexpr int u = U.value, j = ((u >> 1) & 3) * 2 + (u >> 3);
            dmas(ckv, vo + j * 8192 + (u & 1) * 256 + ((n16 ^ ((g4 << 2) | (j & 3))) * 16), LA_IMG + (u & 1) * 65536 + (32 * w + 4 * j) * 256);
        };
        u64 tag;
        Tag tg;
        const bool has_mlp = (ia & 7) != 0;
        const int mi = has_mlp ? 14 * xc + ia - 1 - (ia >> 3) : 0;
        const bool do_g1 = has_mlp && w < 7;
        v4u ga[2], gb[2];
        {
            const int w7 = w < 7 ? w : 6;
            v4u xv, qa[4];
            const int r0 = 56 * (ai & 7) + 8 * w7 + g4;
            const int g0 = 112 * (mi & 3) + 16 * w7 + 4 * (n16 >> 3) + g4;
            const int sh = (lane & 1) ? 0 : 16, rj = (lane >> 1) & 3;
            auto gemv_b = [&](unsigned xb) {
                const unsigned xs = xb >> sh;
                const v4u bf = {rj == 0 ? xs : 0u, rj == 1 ? xs : 0u, rj == 2 ? xs : 0u, rj == 3 ? xs : 0u};
                return bf;
            };
            if (__builtin_expect(w < 7, 1)) {
                xv = *(const GAS v4u*)((const GAS char*)x + (lane < 56 ? lane : 0) * 16);
                const unsigned oq = r0 * 8192 + (ai >> 3) * 512 + n16 * 16;
#pragma unroll
                for (int u = 0; u < 4; ++u) qa[u] = ldgo(wq, oq + (u >> 1) * 4 * 8192 + (u & 1) * 256);
                PIN();
                float xf[8] = {bflo(xv.x), bfhi(xv.x), bflo(xv.y), bfhi(xv.y), bflo(xv.z), bfhi(xv.z), bflo(xv.w), bfhi(xv.w)};
                const float s1 = ((xf[0] + xf[1]) + (xf[2] + xf[3])) + ((xf[4] + xf[5]) + (xf[6] + xf[7]));
                const float mean = wave_sum(lane < 56 ? s1 : 0.f) * (1.f / 448.f);
                float s2 = 0.f;
#pragma unroll
                for (int j = 0; j < 8; ++j) { const float d = xf[j] - mean; s2 += d * d; }
                const float var = wave_sum(lane < 56 ? s2 : 0.f) * (1.f / 448.f);
                const float mb = rbf(mean);
                const float den = rbf(__builtin_amdgcn_sqrtf(rbf(rbf(var) + 1e-5f)));
                const float rden = __builtin_amdgcn_rcpf(den);
                if (lane < 56) {
                    f32x4 o0, o1;
#pragma unroll
                    for (int j = 0; j < 4; j += 2) {
                        const unsigned c0 = pk2(xf[j] - mb, xf[j + 1] - mb), c1 = pk2(xf[4 + j] - mb, xf[5 + j] - mb);
                        const unsigned n0 = pk2(bflo(c0) * rden, bfhi(c0) * rden), n1 = pk2(bflo(c1) * rden, bfhi(c1) * rden);
                        o0[j] = bflo(n0); o0[j + 1] = bfhi(n0); o1[j] = bflo(n1); o1[j + 1] = bfhi(n1);
                    }
                    *(LAS f32x4*)(L + LA_XN + lane * 32) = o0;
                    *(LAS f32x4*)(L + LA_XN + lane * 32 + 16) = o1;
                }
                COMPILER_FENCE();
                unsigned xm[2], gm[2];
#pragma unroll
                for (int u = 0; u < 2; ++u) { xm[u] = *(LAS unsigned*)(L + LA_XN + (r0 + 4 * u) * 4); gm[u] = *(LAS unsigned*)(L + LA_XN + (g0 + 8 * u) * 4); }
                const f32x4 z = {0.f, 0.f, 0.f, 0.f};
                const v4u b0 = gemv_b(xm[0]), b1 = gemv_b(xm[1]);
                const f32x4 d0 = mfma(qa[2], b1, mfma(qa[0], b0, z)), d1 = mfma(qa[3], b1, mfma(qa[1], b0, z));
                const f32x4 d = n16 < 8 ? d0 : d1;
                LAS float* qdst = (LAS float*)(L + LA_QP + w * 1024 + ((n16 >> 3) * 128 + 32 * g4 + (n16 & 7)) * 4);
                qdst[0] = d.x; qdst[8] = d.y; qdst[16] = d.z; qdst[24] = d.w;
                gb[0] = gemv_b(gm[0]); gb[1] = gemv_b(gm[1]);
            } else {
                __builtin_amdgcn_s_sleep(4);
            }
            asm volatile("" : "+s"(epoch));
            tag = epoch * 6364136223846793005ull + 1442695040888963407ull;
            tg = make_tag(tag);
            tile_req(IC<0>{});
            bar();
            if (w < 2) {
                f32x2 a[7];
#pragma unroll
                for (int v = 0; v < 7; ++v) a[v] = *(LAS f32x2*)(L + LA_QP + v * 1024 + tid * 8);
                const f32x2 qv = ((a[0] + a[1]) + (a[2] + a[3])) + ((a[4] + a[5]) + a[6]);
                const float ax = qv.x, ay = qv.y;
                v4u gr = {(f2u(ax) >> 8) | tg.t24, tg.th, (f2u(ay) >> 8) | tg.t24, tg.th};
                stsc(rs, WS_Q1 + ai * 2048 + tid * 16, gr);
            } else {
                __builtin_amdgcn_s_sleep(3);
            }
            PIN();
            const unsigned og = g0 * 3584 + (mi >> 2) * 128 + (lane & 7) * 16;
            if (do_g1) { ga[0] = ld_hidden(w1, og); ga[1] = ld_hidden(w1, og + 8 * 3584); }
            PIN();
        }
        const int off3 = WS_S3 + kg * 65536 + w * 2048 + lane * 16;
        int off4 = WS_P4 + ((2 * w + (lane >> 5)) * 32 + kg) * 2048 + cq * 512 + (lane & 31) * 16;
        constexpr int G1_AT = A_WIN + 2;
        static_for<1, G1_AT>([&](auto U) { wait_vm<A_WIN - 1>(); tile_req(U); });
        if (do_g1) {
            asm volatile("s_waitcnt vmcnt(%2)" : "+v"(ga[0]), "+v"(ga[1]) : "i"(A_WIN) : "memory");
            const f32x4 z = {0.f, 0.f, 0.f, 0.f};
            const f32x4 dg = mfma(ga[1], gb[1], mfma(ga[0], gb[0], z));
            LAS float* pdst = (LAS float*)(L + LA_GP + (2 * w + (g4 >> 1)) * 256 + (32 * (g4 & 1) + (n16 & 7)) * 4);
            if ((g4 >> 1) == (n16 >> 3)) { pdst[0] = dg.x; pdst[8] = dg.y; pdst[16] = dg.z; pdst[24] = dg.w; }
            lds_post(L + L_CNT, lane);
        }
        if (w == 7 && lane < 4) {
            const unsigned o = lane < 2 ? WS_H4 + (kg * 4 + cq) * 256 + lane * 128 : (lane == 2 ? WS_H3 + (kg * 4 + cq) * 128 : WS_ST4 + kg * 128);
            touch_sc(ws, o);
        }
        static_for<G1_AT, 16>([&](auto U) { wait_vm<A_WIN - 1>(); tile_req(U); });
        if (w == 7 && has_mlp) {
            lds_wait(L + L_CNT, 7u);
            f32x2 a = {0.f, 0.f};
#pragma unroll
            for (int v = 0; v < 14; ++v) a += *(LAS f32x2*)(L + LA_GP + v * 256 + (lane & 31) * 8);
            const float ax = a.x, ay = a.y;
            v4u gr = {(f2u(ax) >> 8) | tg.t24, tg.th, (f2u(ay) >> 8) | tg.t24, tg.th};
            stsc(rs, lane < 32 ? WS_G1 + mi * 512 + lane * 16 : OOB, gr);
        }
        const int offq = WS_Q2 + 2 * w * 4096 + cq * 1024 + lane * 16;
        v4u a[16];
        auto read_a = [&](int T) {
            const int row = 8 * (n16 >> 2) + 4 * T + (n16 & 3), sw = ((n16 & 3) << 2) | ((2 * (n16 >> 2) + T) & 3);
#pragma unroll
            for (int ks = 0; ks < 8; ++ks)
                a[T * 8 + ks] = lds128(L + LA_IMG + (ks >> 2) * 65536 + (32 * w + row) * 256 + (((4 * (ks & 3) + g4) ^ sw) * 16));
        };
        wait_vm<3>();
        v4u qv0 = ldsc(rs, offq), qv1 = ldsc(rs, offq + 4096);
        PIN();
        read_a(0);
        PIN();
        wait_vm<2>();
        read_a(1);
        LAS unsigned char* qsrc = L + LA_Q + n16 * 64 + ((g4 ^ ((-(n16 >> 2)) & 3)) * 16);
        PIN();
        if (__builtin_expect(!__all((tagdiff(qv0, tg.lo) | tagdiff(qv1, tg.lo)) == 0u), 0)) {
#pragma nounroll
            for (int spin = 0; spin < SPIN_CAP; ++spin) {
                if (!__all(tagdiff(qv0, tg.lo) == 0u)) qv0 = ldsc(rs, offq);
                if (!__all(tagdiff(qv1, tg.lo) == 0u)) qv1 = ldsc(rs, offq + 4096);
                if (__all((tagdiff(qv0, tg.lo) | tagdiff(qv1, tg.lo)) == 0u)) break;
                COMPILER_FENCE();
            }
        }
        {
            LAS unsigned char* dst = L + LA_Q + (lane >> 3) * 1024 + w * 128 + ((((lane >> 1) & 3) ^ ((-(w >> 1)) & 3)) * 16) + (lane & 1) * 8;
            v2u p0 = {qv0.x, qv0.z}, p1 = {qv1.x, qv1.z};
            *(LAS v2u*)dst = p0;
            *(LAS v2u*)(dst + 64) = p1;
        }
        bar();
        f32x4 s0 = {0.f, 0.f, 0.f, 0.f}, s1 = {0.f, 0.f, 0.f, 0.f};
        const v4u T8 = {tg.lo & 255u, (tg.lo >> 8) & 255u, ~tg.lo & 255u, (tg.lo >> 16) & 255u};
        {
            v4u q[8];
#pragma unroll
            for (int ks = 0; ks < 8; ++ks) q[ks] = lds128(qsrc + ks * 1024);
            if (w == 7) post_hint(rs, lane == 0 ? WS_H3 + (kg * 4 + cq) * 128 : OOB, tg);
            PIN();
#pragma unroll
            for (int ks = 0; ks < 8; ++ks) { s0 = mfma(a[ks], q[ks], s0); s1 = mfma(a[8 + ks], q[ks], s1); }
            v4u o0 = __builtin_bit_cast(v4u, s0) & 0xffffff00u, o1 = __builtin_bit_cast(v4u, s1) & 0xffffff00u;
            stsc(rs, off3 + cq * 16384, o0 | T8);
            stsc(rs, off3 + cq * 16384 + 1024, o1 | T8);
            s0 = __builtin_bit_cast(f32x4, o0); s1 = __builtin_bit_cast(f32x4, o1);
        }
        PIN();
        v4u vf[16];
        {
            const int q = n16 >> 2, p = lane & 3;
#pragma unroll
            for (int t = 0; t < 2; ++t)
#pragma unroll
                for (int c = 0; c < 2; ++c) {
                    LAS unsigned char* base = L + LA_IMG + (w >> 2) * 65536 + (8 * g4 + 4 * t + q) * 256 +
                                              (((4 * (w & 3) + 2 * c + (p >> 1)) ^ ((q << 2) | ((2 * g4 + t) & 3))) * 16) + 8 * (p & 1);
#pragma unroll
                    for (int ks = 0; ks < 8; ++ks) {
                        v2u r = trread(base + ks * 8192);
                        if (t == 0) { vf[c * 8 + ks].x = r.x; vf[c * 8 + ks].y = r.y; } else { vf[c * 8 + ks].z = r.x; vf[c * 8 + ks].w = r.y; }
                    }
                }
        }
        LAS unsigned char* pdst = L + LA_PF + w * 1024 + n16 * 64 + ((g4 ^ ((-(n16 >> 2)) & 3)) * 16);
        LAS unsigned char* psrc = L + LA_PF + n16 * 64 + ((g4 ^ ((-(n16 >> 2)) & 3)) * 16);
        LAS unsigned char* mdst = L + LA_MX + n16 * 32 + w * 4;
        LAS unsigned char* msrc = L + LA_MX + n16 * 32;
        LAS unsigned char* tdst = L + LA_TR + n16 * 528 + w * 64 + g4 * 8;
        LAS unsigned char* tsrc = L + LA_TR + (2 * w + (lane >> 5)) * 528 + (lane & 31) * 16;
        int offh = lane < 16 ? WS_H4 + (kg * 4 + cq) * 256 + lane * 16 : OOB;
        int o31 = off3 + (cq ^ 1) * 16384, o32 = off3 + (cq ^ 2) * 16384, o33 = off3 + (cq ^ 3) * 16384;
        asm volatile("" : "+v"(pdst), "+v"(psrc), "+v"(mdst), "+v"(msrc), "+v"(tdst), "+v"(tsrc), "+v"(off4), "+v"(offh), "+v"(o31), "+v"(o32), "+v"(o33));
        PIN();
        if (w == 7) wait_hints(rs, WS_H3 + (kg * 4 + (cq ^ ((lane & 3) | ((lane & 3) == 0)))) * 128, tg);
        bar();
        {
            auto ld = [&](int off) { return ldsc(rs, off); };
            auto low = [](v4u a, v4u b) { const v4u o = a | b; return (o.x | o.y | o.z | o.w) & 255u; };
            v4u p1a = ld(o31), p1b = ld(o31 + 1024), p2a = ld(o32), p2b = ld(o32 + 1024), p3a = ld(o33), p3b = ld(o33 + 1024);
            const v4u SV = {SENT, SENT, SENT, SENT};
            stsc(rs, off4, SV);
            PIN();
            p1a ^= T8; p1b ^= T8; p2a ^= T8; p2b ^= T8; p3a ^= T8; p3b ^= T8;
            if (__builtin_expect(!__all(low(p1a | p2a | p3a, p1b | p2b | p3b) == 0u), 0)) {
#pragma nounroll
                for (int spin = 0; spin < SPIN_CAP; ++spin) {
                    const bool k1 = __all(low(p1a, p1b) == 0u), k2 = __all(low(p2a, p2b) == 0u), k3 = __all(low(p3a, p3b) == 0u);
                    if (k1 && k2 && k3) break;
                    v4u t1a = p1a ^ T8, t1b = p1b ^ T8, t2a = p2a ^ T8, t2b = p2b ^ T8, t3a = p3a ^ T8, t3b = p3b ^ T8;
                    if (!k1) { t1a = ld(o31); t1b = ld(o31 + 1024); }
                    if (!k2) { t2a = ld(o32); t2b = ld(o32 + 1024); }
                    if (!k3) { t3a = ld(o33); t3b = ld(o33 + 1024); }
                    p1a = t1a ^ T8; p1b = t1b ^ T8; p2a = t2a ^ T8; p2b = t2b ^ T8; p3a = t3a ^ T8; p3b = t3b ^ T8;
                    COMPILER_FENCE();
                }
            }
            s0 = (s0 + __builtin_bit_cast(f32x4, p1a)) + (__builtin_bit_cast(f32x4, p2a) + __builtin_bit_cast(f32x4, p3a));
            s1 = (s1 + __builtin_bit_cast(f32x4, p1b)) + (__builtin_bit_cast(f32x4, p2b) + __builtin_bit_cast(f32x4, p3b));
            float s[8] = {s0.x, s0.y, s0.z, s0.w, s1.x, s1.y, s1.z, s1.w};
            float m = fmx(fmx(fmx(fmx(s[0], s[1]), s[2]), fmx(fmx(s[3], s[4]), s[5])), fmx(s[6], s[7]));
            m = max16(m); m = max32(m);
            const float Mw = __builtin_amdgcn_fmed3f(__builtin_ceilf(m * L2E), -8388608.f, 8388608.f);
#pragma unroll
            for (int j = 0; j < 8; ++j) s[j] = __builtin_amdgcn_exp2f(__builtin_fmaf(s[j], L2E, -Mw));
            const int Jw = (int)Mw << 7;
            *(LAS int*)mdst = Jw;
            v4u p = {pk2(s[0], s[1]), pk2(s[2], s[3]), pk2(s[4], s[5]), pk2(s[6], s[7])};
            *(LAS v4u*)pdst = p;
            PIN();
            wait_vm<0>();
            bar();
            const i32x4 ja = *(LAS i32x4*)msrc, jb = *(LAS i32x4*)(msrc + 16);
            v4u pf[8];
#pragma unroll
            for (int ks = 0; ks < 8; ++ks) pf[ks] = lds128(psrc + ks * 1024);
            PIN();
            float l = ((s[0] + s[1]) + (s[2] + s[3])) + ((s[4] + s[5]) + (s[6] + s[7]));
            l = add16(l, l); l = add32(l, l);
            PIN();
            const int J = max(max(max(ja.x, ja.y), max(ja.z, ja.w)), max(max(jb.x, jb.y), max(jb.z, jb.w)));
            auto shift = [&](int ks) {
                const u16 v = (u16)min((unsigned)(J - (ks < 4 ? ja[ks & 3] : jb[ks & 3])), 0x7f80u);
                const u16x8 sv = {v, v, v, v, v, v, v, v};
                pf[ks] = __builtin_bit_cast(v4u, __builtin_elementwise_sub_sat(__builtin_bit_cast(u16x8, pf[ks]), sv));
            };
            shift(0);
            PIN();
            f32x4 d0 = {0.f, 0.f, 0.f, 0.f}, d1 = {0.f, 0.f, 0.f, 0.f};
#pragma unroll
            for (int ks = 0; ks < 8; ++ks) {
                d0 = mfma(vf[ks], pf[ks], d0); d1 = mfma(vf[8 + ks], pf[ks], d1);
                PIN();
                if (ks < 7) shift(ks + 1);
                if (ks == 0 && w == 7) { v4u h = {tg.lo, tg.hi, f2u((float)(J >> 7)), tg.lo}; stsc(rs, offh, h); }
                if (ks == 1) *(LAS float*)(mdst + 512) = __builtin_ldexpf(l, (Jw - J) >> 7);
                PIN();
            }
            v2u o0 = {nosent(pk2(d0.x, d0.y)), nosent(pk2(d0.z, d0.w))}, o1 = {nosent(pk2(d1.x, d1.y)), nosent(pk2(d1.z, d1.w))};
            *(LAS v2u*)tdst = o0;
            *(LAS v2u*)(tdst + 32) = o1;
        }
        bar();
        stsc(rs, off4, lds128(tsrc));
        if (w == 0 && cq == 0) {
            const f32x4 la = *(LAS f32x4*)(msrc + 512), lb = *(LAS f32x4*)(msrc + 528);
            const float l = ((la.x + la.y) + (la.z + la.w)) + ((lb.x + lb.y) + (lb.z + lb.w));
            v2u st = {f2u(l), tg.lo};
            stsc2(rs, lane < 16 ? WS_ST4 + kg * 128 + lane * 8 : OOB, st);
        }
        PIN();
        if (has_mlp) {
            const int offg = WS_G1 + (mi >> 2) * 2048 + (lane & 3) * 512 + ((mi & 3) * 16 + 2 * w + ((lane >> 2) & 1)) * 8;
            const unsigned o2 = (mi * 16 + 2 * w) * 896 + (lane < 56 ? lane : 0) * 16;
            const v4u m2[2] = {ldgo(w2, o2), ldgo(w2, o2 + 896)};
            const unsigned b1v = *(const GAS u16*)(b1 + mi * 16 + 2 * w + ((lane >> 2) & 1));
            PIN();
            v2u gv;
#pragma nounroll
            for (int spin = 0; spin < SPIN_CAP; ++spin) {
                gv = ldsc2(rs, offg);
                if (__all((gv.x ^ tg.t24) < 0x1000000u && gv.y == tg.th)) break;
                __builtin_amdgcn_s_sleep(4);
                COMPILER_FENCE();
            }
            float hv = u2f(gv.x << 8);
            hv += dpp<DPP_X1>(hv); hv += dpp<DPP_X2>(hv);
            hv = rbf(rbf(hv) + bflo(b1v));
            hv = hv > 0.f ? hv : 0.f;
            const unsigned hb = f2u(rbf(hv * hv));
            float acc[8];
#pragma unroll
            for (int j = 0; j < 8; ++j) acc[j] = 0.f;
#pragma unroll
            for (int u = 0; u < 2; ++u) fma8(acc, u2f(__builtin_amdgcn_readlane(hb, 4 * u)), m2[u]);
            if (lane < 56) {
                f32x4 a0 = {acc[0], acc[1], acc[2], acc[3]}, a1 = {acc[4], acc[5], acc[6], acc[7]};
                *(LAS f32x4*)(L + LA_IMG + w * 1792 + lane * 32) = a0;
                *(LAS f32x4*)(L + LA_IMG + w * 1792 + lane * 32 + 16) = a1;
            }
            bar();
            if (w < 4) {
                f32x2 a = {0.f, 0.f};
#pragma unroll
                for (int v = 0; v < 8; ++v) a += *(LAS f32x2*)(L + LA_IMG + v * 1792 + tid * 8);
                const float a0 = a.x, a1 = a.y;
                v4u o4 = {f2u(a0), tg.lo, f2u(a1), tg.lo};
                stsc(rs, tid < 224 ? WS_M5 + (tid >> 3) * (112 * 128) + mi * 128 + (tid & 7) * 16 : OOB, o4);
            }
        }
    } else {
        const int iw = ii - 16;
        const int hd = 2 * xc + (iw >> 3), ch = (iw >> 2) & 1, dq = iw & 3, wi = xc * 16 + iw;
        const int r8 = lane >> 3, pos = lane & 7;
        const int fi = (iw & 12) == 8 ? (iw & 3) * 8 + xc : 99;
        const unsigned vuv = (512 * ch + 64 * w + r8) * 8192 + hd * 512 + dq * 128;
        auto uv_req = [&](int k) {
            dmas(wuv, vuv + k * 65536 + ((pos ^ ((k & 3) << 1)) * 16), LW_UV + (64 * w + 8 * k) * 128);
        };
        u64 tag;
        Tag tg;
        {
            v4u wk[8];
            const unsigned ok = (128 * (iw & 7) + 16 * w + (n16 >> 2)) * 8192 + hd * 512 + g4 * 64 + (n16 & 3) * 16;
            __builtin_amdgcn_s_sleep(W_DELAY);
#pragma unroll
            for (int u = 0; u < 8; ++u) wk[u] = ldgo(wuk, ok + (u >> 1) * 4 * 8192 + (u & 1) * 256);
            PIN();
            asm volatile("" : "+s"(epoch));
            tag = epoch * 6364136223846793005ull + 1442695040888963407ull;
            tg = make_tag(tag);
            LAS unsigned char* qfs[4];
#pragma unroll
            for (int a = 0; a < 4; ++a) qfs[a] = L + ((n16 >> 2) == a ? LW_QV : L_Z) + g4 * 64 + (n16 & 3) * 16;
            int offq2 = (lane & 51) == 0 ? WS_Q2 + wi * 512 + w * 64 + n16 * 4 : OOB;
            asm volatile("" : "+v"(qfs[0]), "+v"(qfs[1]), "+v"(qfs[2]), "+v"(qfs[3]), "+v"(offq2));
            wait_vm<0>();
            bar();
            {
                const int off1 = WS_Q1 + hd * 16384 + g4 * 2048 + w * 256 + n16 * 16;
                v4u v0 = ldsc(rs, off1), v1 = ldsc(rs, off1 + 8192);
                auto bad = [&]() { return (((v0.x ^ tg.t24) | (v0.z ^ tg.t24) | (v1.x ^ tg.t24) | (v1.z ^ tg.t24)) >> 24) | tagdiff(v0, tg.th) | tagdiff(v1, tg.th); };
                if (__builtin_expect(!__all(bad() == 0u), 0)) {
#pragma nounroll
                    for (int spin = 0; spin < SPIN_CAP; ++spin) {
                        v0 = ldsc(rs, off1); v1 = ldsc(rs, off1 + 8192);
                        if (__all(bad() == 0u)) break;
                        COMPILER_FENCE();
                    }
                }
                float t = add32(u2f(v0.x << 8) + u2f(v1.x << 8), u2f(v0.z << 8) + u2f(v1.z << 8));
                t = add16(t, t);
                *(LAS u16*)(L + LW_QV + w * 64 + n16 * 4 + (lane >> 5) * 2) = (u16)pk2(t, t);
            }
            bar();
            v4u qf[8];
#pragma unroll
            for (int u = 0; u < 8; ++u) qf[u] = lds128(qfs[u >> 1] + (u & 1) * 256);
            f32x4 d0 = {0.f, 0.f, 0.f, 0.f}, d1 = {0.f, 0.f, 0.f, 0.f};
#pragma unroll
            for (int u = 0; u < 8; u += 2) { d0 = mfma(wk[u], qf[u], d0); d1 = mfma(wk[u + 1], qf[u + 1], d1); }
            d0 += d1;
            float v = (lane & 2) ? ((lane & 1) ? d0.w : d0.z) : ((lane & 1) ? d0.y : d0.x);
            v += dpp<DPP_X1>(v); v += dpp<DPP_X2>(v);
            v *= 0.0625f;
            unsigned e0 = f2u(v), e1 = e0;
            swap16(e0, e1);
            unsigned p0 = pk2(u2f(e0), u2f(e1)), p1 = p0;
            swap32(p0, p1);
            v4u gr = {p0, tg.lo, p1, tg.lo};
            stsc(rs, offq2, gr);
            __builtin_amdgcn_s_barrier();
            PIN();
        }
        const int fc = (fi < 28 ? fi : 0) * 16 + n16;
        const unsigned xb = *(const GAS u16*)(x + fc), b2b = *(const GAS u16*)(b2 + fc);
        PIN();
        const unsigned so = (hd * 256 + dq * 64 + 32 * ch + 4 * w) * 896 + lane * 16, sx = so ^ ((hd * 256 + dq * 64 + 32 * (ch ^ 1) + 4 * w) * 896 + lane * 16);
        const int lo = LW_WO + (32 * ch + 4 * w) * 928 + (4 * ch + (w >> 1)) * 128, lx = lo ^ (LW_WO + (32 * (ch ^ 1) + 4 * w) * 928 + (4 * (ch ^ 1) + (w >> 1)) * 128);
        {
            const bool slow = (iw & 11) == 3 || (wi & 123) == 123;
            if (w >= 4) touch(ckv, (1024 * xc + 64 * iw + 16 * (w & 3) + 4 + 8 * (lane >> 5) + ((lane >> 3) & 3)) * 2048 + (lane & 4) * 256 + (lane & 3) * 128);
            const int nslow = slow ? 0 : T_SLOWN;
#pragma nounroll
            for (int k = 0; k < 8; ++k) {
                if (2 * k < nslow) wait_vm<0>(); else wait_vm<T_WIN - 1>();
                uv_req(k);
                if (2 * k + 1 < nslow) wait_vm<0>(); else wait_vm<T_WIN - 1>();
                if (lane < 56) dmas<false>(wo, (k < 4 ? so : so ^ sx) + (k & 3) * 896, (k < 4 ? lo : lo ^ lx) + (k & 3) * 928);
            }
        }
        const int rl = 4 * (n16 >> 3) + g4;
        const int off4 = WS_P4 + (hd * 32 + rl) * 2048 + ch * 1024 + w * 128 + pos * 16;
        const int soff = WS_ST4 + (w == 7 ? (lane & 31) : 0) * 128 + hd * 8;
        LAS unsigned char* bdst = L + LW_UV + w * 8192 + ((g4 >> 1) == (n16 >> 3) ? (g4 >> 1) * 288 : 576) + (g4 & 1) * 64 + (2 * (n16 & 3) + ((n16 >> 2) & 1)) * 8;
        LAS unsigned char* bsrc[4];
#pragma unroll
        for (int ct = 0; ct < 4; ++ct) bsrc[ct] = L + ((n16 >> 2) == ct ? LW_UV + w * 8192 + (n16 & 3) * 144 : L_Z) + g4 * 16;
        LAS unsigned char* cdst = L + LW_CT + w * 144 + lane * 2;
        LAS unsigned char* csrc = L + LW_CT + n16 * 144 + g4 * 16;
        int off5 = ((lane & 1) == 0 && w < 7) ? WS_P5 + (4 * w + g4) * 16384 + wi * 128 + n16 * 8 : OOB;
        asm volatile("" : "+v"(bdst), "+v"(bsrc[0]), "+v"(bsrc[1]), "+v"(bsrc[2]), "+v"(bsrc[3]), "+v"(cdst), "+v"(csrc), "+v"(off5));
        PIN();
        wait_vm<0>();
        if (w == 0) {
            const int offh = WS_H4 + ((lane >> 1) * 4 + 2 * ch + (lane & 1)) * 256 + hd * 16;
            v4u hv;
#pragma nounroll
            for (int spin = 0; spin < SPIN_CAP; ++spin) {
                hv = ldsc(rs, offh);
                if (__all(((hv.x ^ tg.lo) | (hv.y ^ tg.hi) | (hv.w ^ tg.lo)) == 0u)) break;
                COMPILER_FENCE();
            }
            const float mk = u2f(hv.z);
            float M = fmx(mk, dpp<DPP_X2>(mk)); M = fmx(M, dpp<DPP_ROR4>(M)); M = fmx(M, dpp<DPP_ROR8>(M));
            M = max16(M); M = max32(M);
            *(LAS float*)(L + LW_FS + (lane >> 1) * 4) = __builtin_ldexpf(1.0f, (int)(mk - M));
        }
        bar();
        v4u uf[8], of[8];
        LAS unsigned char* ob0 = L + LW_WO + (8 * g4 + (n16 >> 2)) * 928 + g4 * 128 + (w < 7 ? w : 6) * 128 + (lane & 3) * 8;
        auto read_of = [&](int i, int ks) {
#pragma unroll
            for (int ct = 0; ct < 4; ++ct) {
                const v2u a = trread(ob0 + ks * (32 * 928 + 512) + ct * 32), c = trread(ob0 + ks * (32 * 928 + 512) + 4 * 928 + ct * 32);
                of[i + ct] = v4u{a.x, a.y, c.x, c.y};
            }
        };
        {
            v4u c[4];
#pragma unroll
            for (int u = 0; u < 4; ++u) c[u] = ldsc(rs, off4 + u * 8 * 2048);
            v2u st = ldsc2(rs, soff);
            PIN();
            const int sh = (lane & 1) ? 0 : 16, rj = (lane >> 1) & 3;
            v4u fb[4];
#pragma unroll
            for (int u = 0; u < 4; ++u) {
                const unsigned xs = (*(LAS unsigned*)(L + LW_FS + (8 * u + rl) * 4) & 0xffff0000u) >> sh;
                fb[u] = v4u{rj == 0 ? xs : 0u, rj == 1 ? xs : 0u, rj == 2 ? xs : 0u, rj == 3 ? xs : 0u};
            }
            const float fk = *(LAS float*)(L + LW_FS + (lane & 31) * 4);
            {
                const int q = n16 >> 2, p = lane & 3;
#pragma unroll
                for (int t = 0; t < 2; ++t) {
                    LAS unsigned char* ub = L + LW_UV + (64 * w + 8 * q + g4 + 4 * t) * 128 + 8 * (p & 1);
#pragma unroll
                    for (int ks = 0; ks < 2; ++ks)
#pragma unroll
                        for (int ct = 0; ct < 4; ++ct) {
                            v2u r = trread(ub + ks * 4096 + (((2 * ct + (p >> 1)) ^ (q << 1)) * 16));
                            if (t == 0) { uf[ks * 4 + ct].x = r.x; uf[ks * 4 + ct].y = r.y; } else { uf[ks * 4 + ct].z = r.x; uf[ks * 4 + ct].w = r.y; }
                        }
                }
                read_of(0, ch);
                read_of(4, ch ^ 1);
            }
            PIN();
            if (__builtin_expect(!__all(max(max(umax4(c[0]), umax4(c[1])), max(umax4(c[2]), umax4(c[3]))) != SENT && st.y == tg.lo), 0)) {
#pragma nounroll
                for (int spin = 0; spin < SPIN_CAP; ++spin) {
                    bool all = true;
#pragma unroll
                    for (int u = 0; u < 4; ++u) if (!__all(umax4(c[u]) != SENT)) { all = false; c[u] = ldsc(rs, off4 + u * 8 * 2048); }
                    if (!__all(st.y == tg.lo)) { all = false; st = ldsc2(rs, soff); }
                    if (all) break;
                    COMPILER_FENCE();
                }
            }
            const f32x4 z = {0.f, 0.f, 0.f, 0.f};
            const f32x4 e = mfma(c[1], fb[1], mfma(c[0], fb[0], z)) + mfma(c[3], fb[3], mfma(c[2], fb[2], z));
            const unsigned h0 = pk2(e.x, e.y), h1 = pk2(e.z, e.w);
            v2u hi = {h0, h1}, lo = {pk2(e.x - bflo(h0), e.y - bfhi(h0)), pk2(e.z - bflo(h1), e.w - bfhi(h1))};
            *(LAS v2u*)bdst = hi;
            *(LAS v2u*)(bdst + 144) = lo;
            COMPILER_FENCE();
            v4u af[8];
#pragma unroll
            for (int i = 0; i < 8; ++i) af[i] = lds128(bsrc[i & 3] + (i >> 2) * 64);
            PIN();
            float Ls = fk * u2f(st.x);
            Ls += dpp<DPP_X1>(Ls); Ls += dpp<DPP_X2>(Ls); Ls += dpp<DPP_ROR4>(Ls); Ls += dpp<DPP_ROR8>(Ls);
            Ls = add16(Ls, Ls);
            if (w == 7) *(LAS float*)(L + LW_FS + 128) = __builtin_amdgcn_rcpf(Ls);
            PIN();
            f32x4 d0 = z, d1 = z;
#pragma unroll
            for (int ct = 0; ct < 4; ++ct) { d0 = mfma(af[ct], uf[ct], d0); d1 = mfma(af[4 + ct], uf[4 + ct], d1); }
            d0 += d1;
            const float cv = (d0.x + d0.y) + (d0.z + d0.w);
            const float chi = rbf(cv);
            const unsigned pk = pk2(chi, cv - chi);
            *(LAS u16*)cdst = (u16)pk;
            *(LAS u16*)(cdst + 8 * 144) = (u16)(pk >> 16);
        }
        bar();
        {
            const v4u a0 = lds128(csrc + ch * 64), a1 = lds128(csrc + (ch ^ 1) * 64);
            const float rls = *(LAS float*)(L + LW_FS + 128);
            f32x4 d[4];
#pragma unroll
            for (int ct = 0; ct < 4; ++ct) {
                f32x4 z = {0.f, 0.f, 0.f, 0.f};
                d[ct] = mfma(a0, of[ct], z);
            }
#pragma unroll
            for (int ct = 0; ct < 4; ++ct) d[ct] = mfma(a1, of[4 + ct], d[ct]);
            float sx[4];
#pragma unroll
            for (int ct = 0; ct < 4; ++ct) sx[ct] = (d[ct].x + d[ct].y) + (d[ct].z + d[ct].w);
            const float r0 = add32(sx[0], sx[2]), r1 = add32(sx[1], sx[3]);
            const float sv = add16(r0, r1) * rls;
            v4u o = {f2u(sv), tg.lo, f2u(dpp<DPP_X1>(sv)), tg.lo};
            stsc(rs, off5, o);
        }
        PIN();
        if (fi < 28) {
            const int wq7 = w < 7 ? w : 6;
            const int offm = WS_M5 + fi * (112 * 128) + wq7 * 2048 + lane * 16, offs = WS_P5 + fi * 16384 + w * 2048 + lane * 16;
            LAS unsigned char* fdst = L + LW_FR + ((lane & 7) * 2 + (lane >> 5)) * 32 + w * 4;
            v4u a0 = ldsc(rs, offm), a1 = ldsc(rs, offm + 1024);
            PIN();
            __builtin_amdgcn_s_sleep(F_SLEEP);
            v4u c0 = ldsc(rs, offs), c1 = ldsc(rs, offs + 1024);
            if (__builtin_expect(!__all((tagdiff(a0, tg.lo) | tagdiff(a1, tg.lo) | tagdiff(c0, tg.lo) | tagdiff(c1, tg.lo)) == 0u), 0)) {
#pragma nounroll
                for (int spin = 0; spin < SPIN_CAP; ++spin) {
                    if (!__all(tagdiff(a0, tg.lo) == 0u)) a0 = ldsc(rs, offm);
                    if (!__all(tagdiff(a1, tg.lo) == 0u)) a1 = ldsc(rs, offm + 1024);
                    if (!__all(tagdiff(c0, tg.lo) == 0u)) c0 = ldsc(rs, offs);
                    if (!__all(tagdiff(c1, tg.lo) == 0u)) c1 = ldsc(rs, offs + 1024);
                    if (__all((tagdiff(a0, tg.lo) | tagdiff(a1, tg.lo) | tagdiff(c0, tg.lo) | tagdiff(c1, tg.lo)) == 0u)) break;
                    COMPILER_FENCE();
                }
            }
            {
                const float t0 = add32(u2f(a0.x) + u2f(a1.x), u2f(a0.z) + u2f(a1.z)), t1 = add32(u2f(c0.x) + u2f(c1.x), u2f(c0.z) + u2f(c1.z));
                float t = add16(w < 7 ? t0 : 0.f, t1);
                t += dpp<DPP_ROR8>(t);
                *(LAS float*)(fdst + (g4 & 1) * 512) = t;
            }
            bar();
            if (w == 0) {
                const f32x4 sa = *(LAS f32x4*)(L + LW_FR + (g4 & 1) * 512 + n16 * 32), sb = *(LAS f32x4*)(L + LW_FR + (g4 & 1) * 512 + n16 * 32 + 16);
                unsigned sm = f2u(((sa.x + sa.y) + (sa.z + sa.w)) + ((sb.x + sb.y) + (sb.z + sb.w))), sv = sm;
                swap16(sm, sv);
                const float o2 = rbf(rbf(u2f(sm)) + bflo(b2b));
                const float gate = rbf(o2 * __builtin_amdgcn_rcpf(1.0f + __builtin_amdgcn_exp2f(-o2 * L2E)));
                const float o = bflo(xb) + u2f(sv) * gate;
                if (lane < 16) *(GAS u16*)(out + fi * 16 + lane) = (u16)pk2(o, o);
                if (fi == 0 && lane == 0) *(GAS u64*)(ws + WS_E) = tag;
            }
        }
    }
}

extern "C" void kernel_launch(void* const* d_in, const int* in_sizes, int n_in, void* d_out, int out_size, void* d_ws, size_t ws_size,
                              hipStream_t stream) {
    (void)hipFuncSetAttribute((const void*)mla_decode_kernel, hipFuncAttributeMaxDynamicSharedMemorySize, LDS_BYTES);
    mla_decode_kernel<<<256, 512, LDS_BYTES, stream>>>((const u16*)d_in[0], (const u16*)d_in[1], (const u16*)d_in[2], (const u16*)d_in[3],
                                                       (const u16*)d_in[4], (const u16*)d_in[5], (const u16*)d_in[7], (const u16*)d_in[8],
                                                       (const u16*)d_in[9], (const u16*)d_in[10], (u16*)d_out, (unsigned char*)d_ws);
}
```
